```python
import torch

def _softmax(x, axis=-1):
    return torch.softmax(x, dim=axis)
def _gelu(x):
    return torch.nn.functional.gelu(x, approximate="tanh")
def _silu(x):
    return torch.nn.functional.silu(x)
def _sigmoid(x):
    return torch.sigmoid(x)
def _layernorm(x, axis=-1, eps=1e-5):
    m = x.mean(dim=axis, keepdim=True)
    v = x.var(dim=axis, keepdim=True, unbiased=False)
    return (x - m) / torch.sqrt(v + eps)
def _scatter_add(tgt, idx, src, axis=0):
    return tgt.clone().index_add_(axis, idx, src)
def _segment_sum(src, seg, n):
    out = torch.zeros((n,) + src.shape[1:], dtype=src.dtype)
    return out.index_add_(0, seg, src)
def _pad(x, pw):
    flat = tuple(v for pair in reversed(pw) for v in pair)
    return torch.nn.functional.pad(x, flat)

kernel_name = 'gated_mix__mha_block__geglu_ffn__prompt'

def setup_inputs():

    _w = (lambda a: (lambda t: t.to(torch.bfloat16) if t.is_floating_point() else t)(torch.as_tensor(a)))
    def _A_setup():
        B, S, D, H = 1, 2048, 448, 16
        return {
            "x": _w((lambda *s: torch.randn(*s, dtype=torch.bfloat16) * 0.05)(B, S, D)),
            "wqkv": _w((lambda *s: torch.randn(*s, dtype=torch.bfloat16) * 0.8)(D, 3 * D)),
            "wo": _w((lambda *s: torch.randn(*s, dtype=torch.bfloat16) * 0.05)(D, D)),
        }
    _A_in = _A_setup()

    def _B_setup():
        B, S, D, F = 1, 2048, 448, 1792
        return {
            "x": _w((lambda *s: torch.randn(*s, dtype=torch.bfloat16) * 0.05)(B, S, D)),
            "w_gate": _w((lambda *s: torch.randn(*s, dtype=torch.bfloat16) * 0.1)(D, F)),
            "w_up": _w((lambda *s: torch.randn(*s, dtype=torch.bfloat16) * 0.1)(D, F)),
            "w_down": _w((lambda *s: torch.randn(*s, dtype=torch.bfloat16) * 0.1)(F, D)),
        }
    _B_in = _B_setup()

    return {
        "A_x": _A_in["x"],
        "A_wqkv": _A_in["wqkv"],
        "A_wo": _A_in["wo"],
        "B_x": _B_in["x"],
        "B_w_gate": _B_in["w_gate"],
        "B_w_up": _B_in["w_up"],
        "B_w_down": _B_in["w_down"],
        "gate_w": _w((lambda *s: torch.randn(*s, dtype=torch.bfloat16) * 0.05)(448) * 16.0),
    }

def reference(A_x, A_wqkv, A_wo, B_x, B_w_gate, B_w_up, B_w_down, gate_w):
    def _A_ref(x, wqkv, wo):
        B, S, D = x.shape
        H = 16
        Dh = D // H
        qkv = (lambda x, *d: x.permute(*d))((x @ wqkv).reshape(B, S, 3, H, Dh), 2, 0, 3, 1, 4)
        q, k, v = qkv[0], qkv[1], qkv[2]
        scores = torch.einsum("bhqd,bhkd->bhqk", q, k) * (Dh ** -0.5)
        p = _softmax(scores, axis=-1)
        o = torch.einsum("bhqk,bhkd->bhqd", p, v)
        return (lambda x, *d: x.permute(*d))(o, 0, 2, 1, 3).reshape(B, S, D) @ wo

    def _B_ref(x, w_gate, w_up, w_down):
        return (_gelu(x @ w_gate) * (x @ w_up)) @ w_down

    x = A_x
    g = _sigmoid((x * gate_w).sum(-1, keepdims=True))
    a = _A_ref(x, A_wqkv, A_wo)
    b = _B_ref(x, B_w_gate, B_w_up, B_w_down)
    return g * a + (1 - g) * b
```

```cpp
#include <hip/hip_runtime.h>
#include <hip/hip_bf16.h>
#include <utility>

typedef short bf16x8 __attribute__((ext_vector_type(8)));
typedef short s16x4 __attribute__((ext_vector_type(4)));
typedef float f32x16 __attribute__((ext_vector_type(16)));
typedef float f32x4 __attribute__((ext_vector_type(4)));
typedef unsigned u32x4 __attribute__((ext_vector_type(4)));
typedef unsigned u32x2 __attribute__((ext_vector_type(2)));
#define LAS __attribute__((address_space(3)))
#define DI __device__ __forceinline__
#define MFMA(a, b, c) __builtin_amdgcn_mfma_f32_32x32x16_bf16((a), (b), (c), 0, 0, 0)
#define SBAR() __builtin_amdgcn_sched_barrier(0)
#define PIN(x) asm volatile("" : "+v"(x))

constexpr int SEQ = 2048, DM = 448, NH = 16, FF = 1792;
constexpr size_t WS_Q = 0, WS_K = 2u << 20, WS_V = 4u << 20, WS_O = 6u << 20, WS_H = 8u << 20, WS_B = 16u << 20, WS_G = 20u << 20;
constexpr float QSCALE = 0.1889822365046136f * 1.4426950408889634f;

template <int... I, class F> DI void static_for_impl(std::integer_sequence<int, I...>, F&& f) { (f(std::integral_constant<int, I>{}), ...); }
template <int N, class F> DI void static_for(F&& f) { static_for_impl(std::make_integer_sequence<int, N>{}, f); }

typedef __bf16 bf16x2_t __attribute__((ext_vector_type(2)));
typedef float f32x2 __attribute__((ext_vector_type(2)));
DI unsigned cvtpk(float lo, float hi) { return __builtin_bit_cast(unsigned, __builtin_convertvector((f32x2){lo, hi}, bf16x2_t)); }
template <int OFF> DI void glds(const char* sbase, unsigned voff, unsigned lds) {
    asm volatile("s_mov_b32 m0, %2\n\ts_nop 0\n\tglobal_load_lds_dwordx4 %0, %1 offset:%3" ::"v"(voff), "s"(sbase), "s"(lds), "i"(OFF) : "memory");
}
DI void gst16(char* sbase, unsigned voff, u32x4 v) { asm volatile("global_store_dwordx4 %0, %1, %2\n\ts_nop 1" ::"v"(voff), "v"(v), "s"(sbase) : "memory"); }
DI void gst8(char* sbase, unsigned voff, u32x2 v) { asm volatile("global_store_dwordx2 %0, %1, %2" ::"v"(voff), "v"(v), "s"(sbase) : "memory"); }
template <int N> DI void wait_bar() { asm volatile("s_waitcnt vmcnt(%0) lgkmcnt(0)\n\ts_barrier" ::"i"(N) : "memory"); }
template <int N> DI void wait_bar_v() { asm volatile("s_waitcnt vmcnt(%0)\n\ts_barrier" ::"i"(N) : "memory"); }
typedef LAS const char* lptr;
DI bf16x8 lds16(lptr p) { return *(const LAS bf16x8*)p; }
typedef short v4i16_t __attribute__((ext_vector_type(4)));
DI bf16x8 ldstr(lptr p) {
    s16x4 lo = __builtin_bit_cast(s16x4, __builtin_amdgcn_ds_read_tr16_b64_v4i16((LAS v4i16_t*)p));
    s16x4 hi = __builtin_bit_cast(s16x4, __builtin_amdgcn_ds_read_tr16_b64_v4i16((LAS v4i16_t*)(p + 512)));
    return __builtin_shufflevector(lo, hi, 0, 1, 2, 3, 4, 5, 6, 7);
}
DI bf16x8 ldstr2(lptr p) {
    s16x4 lo = __builtin_bit_cast(s16x4, __builtin_amdgcn_ds_read_tr16_b64_v4i16((LAS v4i16_t*)p));
    s16x4 hi = __builtin_bit_cast(s16x4, __builtin_amdgcn_ds_read_tr16_b64_v4i16((LAS v4i16_t*)(p + 1024)));
    return __builtin_shufflevector(lo, hi, 0, 1, 2, 3, 4, 5, 6, 7);
}
DI float gelu_mul(float g, float u) {
    const float c1 = -2.302208198f, c2 = -0.1029432397f;
    float t = g * __builtin_fmaf(g * g, c2, c1);
    float r = __builtin_amdgcn_rcpf(1.f + __builtin_amdgcn_exp2f(t));
    return g * u * r;
}

constexpr int k1_need(int j) { return j <= 6 ? 2 * j + 1 : j + 7; }
constexpr int k1_step(int ks) { int p = ks / 28, s = ks % 28; return p < 2 ? p * 7 + s / 4 : 14 + s / 8; }
constexpr bool k1_barpos(int ks) { int p = ks / 28, s = ks % 28; return p < 2 ? (s % 4 == 2) : (s % 8 == (s < 24 ? 4 : 99)); }
constexpr int k1_pair_at(int s) { for (int pi = 0; pi < 8; ++pi) if (s == pi * 7 / 2 + 1) return pi; return -1; }
constexpr int k1_issued_after(int m, int ng) { int v = m < 2 ? 9 : k1_need(m - 2) + 10; return v < ng - 1 ? v : ng - 1; }
constexpr int k1_wait(int m, bool vm) {
    const int ng = vm ? 21 : 25, nks = vm ? 56 : 84, nst = vm ? 14 : 18;
    int cnt = 0, issued = 9; bool seen = false; const int tgt = k1_need(m);
    for (int g = 0; g <= 9; ++g) { if (seen) cnt += 2; if (g == tgt) seen = true; }
    if (m == 0) return cnt;
    for (int ks = 0; ks < nks; ++ks) {
        const int j = k1_step(ks);
        if (k1_barpos(ks) && j + 1 < nst) {
            if (j + 1 == m) return cnt;
            const int upto = k1_issued_after(j + 1, ng);
            for (int g = issued + 1; g <= upto; ++g) { if (seen) cnt += (g == 24 ? 1 : 2); if (g == tgt) seen = true; }
            issued = upto;
        }
        const int pi = k1_pair_at(ks % 28);
        if (!vm && ks >= 28 && (pi == 3 || pi == 7) && seen) cnt += 1;
    }
    return 0;
}

template <bool VM>
DI void k1_body(const char* x, const char* wqkv, const char* wg, const char* wu, const char* gw, char* ws, int rowt, int grp, lptr lds) {
    const int lane = threadIdx.x & 63, w = __builtin_amdgcn_readfirstlane(threadIdx.x >> 6);
    const int tb = w & 3, hh = w >> 2, h = lane >> 5;
    const unsigned lds0 = (unsigned)(size_t)lds;
    constexpr int NG = VM ? 21 : 25, NKS = VM ? 56 : 84, NST = VM ? 14 : 18;
    constexpr int WSTR = VM ? 2688 : 3584;
    const int r = lane >> 3, pos = lane & 7;
    const unsigned vx0 = r * 896 + ((pos ^ (r >> 1)) << 4), vx1 = (8 + r) * 896 + ((pos ^ (4 | (r >> 1))) << 4);
    const unsigned vw = r * WSTR + ((pos ^ (VM ? ((r & 2) << 1) : ((hh << 2) | (r & 2)))) << 4);
    const unsigned vc = r * 2688 + ((pos ^ ((r & 2) << 1)) << 4);
    const char* xb = x + (size_t)(rowt * 128 + 16 * w) * 896;
    const char *wa, *wb;
    if (VM) { const int u0 = (grp - 14) * 4 + hh, u1 = min(u0 + 2, 6); wa = wqkv + 1792 + u0 * 128; wb = wqkv + 1792 + u1 * 128; }
    else { const char* base = hh ? wu : wg; wa = base + grp * 256; wb = wa + 128; }
    wa += (size_t)(16 * tb) * WSTR; wb += (size_t)(16 * tb) * WSTR;
    const char* wc = wqkv + grp * 128;
    const unsigned ldw = lds0 + w * 2048;
    auto issue = [&](auto G_) { constexpr int G = G_; constexpr unsigned sl = (G % 10) * 16384;
        if constexpr (G < 14 && (G & 1) == 0) { const char* b = xb + (G / 2) * 128; glds<0>(b, vx0, ldw + sl); glds<0>(b, vx1, ldw + sl + 1024); }
        else if constexpr (G < 21) { const char* b = (G < 14 ? wa + (size_t)(G / 2) * 64 * WSTR : wb + (size_t)(G - 14) * 64 * WSTR); glds<0>(b, vw, ldw + sl); glds<0>(b + 8 * WSTR, vw, ldw + sl + 1024); }
        else if constexpr (G < 24) { const char* b = wc + (size_t)((G - 21) * 128 + 16 * w) * 2688; glds<0>(b, vc, ldw + sl); glds<0>(b + 8 * 2688, vc, ldw + sl + 1024); }
        else { glds<0>(wc + (size_t)(384 + 8 * w) * 2688, vc, lds0 + w * 1024 + sl); } };
    static_for<10>([&](auto G) { issue(G); });
    const int t = 32 * tb + (lane & 31), gx = (t >> 1) & 7;
    lptr xa[4];
#pragma unroll
    for (int s = 0; s < 4; ++s) xa[s] = lds + t * 128 + (((2 * s + h) ^ gx) << 4);
    const int blk = (lane >> 4) & 1, q = (lane & 15) >> 2, p = lane & 3;
    lptr wad[2];
#pragma unroll
    for (int a = 0; a < 2; ++a) {
        const int stream = VM ? hh : blk, chunk = VM ? 4 * a + 2 * blk + (p >> 1) : 2 * (2 * hh + a) + (p >> 1), swz = VM ? ((q & 2) << 1) : ((blk << 2) | (q & 2));
        wad[a] = lds + stream * 8192 + (8 * h + q) * 128 + ((chunk ^ swz) << 4) + (p & 1) * 8;
    }
    lptr cad = lds + (8 * h + q) * 128 + (((4 * hh + 2 * blk + (p >> 1)) ^ ((q & 2) << 1)) << 4) + (p & 1) * 8;
    bf16x8 xr[28], wf[2][2];
    f32x16 acc[2][2];
    u32x4 pk;
    const int tblk = rowt * 4 + tb;
    auto readf = [&](auto KS_) { constexpr int KS = KS_, P = KS / 28, S = KS % 28, B = KS & 1;
        if constexpr (P == 0) { xr[S] = lds16(xa[S & 3] + ((2 * (S / 4)) % 10) * 16384);
            constexpr int o = ((2 * (S / 4) + 1) % 10) * 16384 + (S & 3) * 2048; wf[B][0] = ldstr(wad[0] + o); wf[B][1] = ldstr(wad[1] + o); }
        else if constexpr (P == 1) { constexpr int o = ((14 + S / 4) % 10) * 16384 + (S & 3) * 2048; wf[B][0] = ldstr(wad[0] + o); wf[B][1] = ldstr(wad[1] + o); }
        else { constexpr int o = ((21 + S / 8) % 10) * 16384 + (S & 7) * 2048; wf[B][0] = ldstr(cad + o); } };
    auto epi = [&](auto PP_, auto PI_) { constexpr int PP = PP_, PI = PI_, A = PI >> 2, J = (PI & 3) * 2;
        if constexpr (!VM) {
            pk[PI & 3] = cvtpk(gelu_mul(acc[PP][A][J], acc[PP][A][8 + J]), gelu_mul(acc[PP][A][J + 1], acc[PP][A][9 + J]));
            if constexpr ((PI & 3) == 3) gst16(ws + WS_H + (size_t)(tblk * 112 + (2 * grp + PP) * 4 + 2 * hh + A) * 1024, lane * 16, pk);
            else PIN(pk);
        } else if constexpr ((PI & 1) == 1) {
            constexpr int S = (PI >> 1) & 1;
            const int unit = (grp - 14) * 4 + 2 * PP + hh;
            if (unit < 7) {
                const int c = unit * 64 + A * 32 + (lane & 31), head = c / 28, d = c - head * 28;
                u32x4 v;
#pragma unroll
                for (int i = 0; i < 4; ++i) v[i] = cvtpk(acc[PP][A][8 * S + 2 * i], acc[PP][A][8 * S + 2 * i + 1]);
                char* vb = ws + WS_V + (size_t)tblk * 2048 + S * 1024;
                gst16(vb, head * 131072 + (h * 32 + d) * 16, v);
                if (d < 4) { const unsigned o = d == 0 ? 0x3F803F80u : 0u; gst16(vb, head * 131072 + (h * 32 + 28 + d) * 16, (u32x4){o, o, o, o}); }
            }
        } };
    wait_bar_v<k1_wait(0, VM)>();
    readf(std::integral_constant<int, 0>{});
    static_for<NKS>([&](auto KS_) { constexpr int KS = KS_, P = KS / 28, S = KS % 28, B = KS & 1, J = k1_step(KS);
        SBAR();
        if constexpr (k1_barpos(KS) && J + 1 < NST) {
            wait_bar_v<k1_wait(J + 1, VM)>();
            constexpr int lo = k1_issued_after(J, NG) + 1, hi = k1_issued_after(J + 1, NG);
            static_for<(hi >= lo ? hi - lo + 1 : 0)>([&](auto I) { issue(std::integral_constant<int, lo + I>{}); });
            if constexpr (VM && J + 1 == 13) { if (grp == 15 && hh == 1) glds<0>(gw, min(lane, 55) * 16, lds0 + 16384 + w * 1024); }
        }
        if constexpr (KS + 1 < NKS) readf(std::integral_constant<int, KS + 1>{});
        const f32x16 z = {};
        if constexpr (P < 2) {
#pragma unroll
            for (int a = 0; a < 2; ++a) { if constexpr (VM) acc[P][a] = MFMA(xr[S], wf[B][a], S == 0 ? z : acc[P][a]); else acc[P][a] = MFMA(wf[B][a], xr[S], S == 0 ? z : acc[P][a]); }
        } else acc[0][0] = MFMA(wf[B][0], xr[S], S == 0 ? z : acc[0][0]);
        if constexpr (P >= 1 && k1_pair_at(S) >= 0) epi(std::integral_constant<int, P - 1>{}, std::integral_constant<int, k1_pair_at(S)>{});
    });
    SBAR();
    if constexpr (VM) {
        static_for<8>([&](auto PI) { epi(std::integral_constant<int, 1>{}, PI); });
        if (grp == 15 && hh == 1) {
            asm volatile("s_waitcnt vmcnt(0)" ::: "memory");
            f32x16 ga = {};
#pragma unroll
            for (int s = 0; s < 28; ++s) ga = MFMA(lds16(lds + 16384 + w * 1024 + s * 32 + h * 16), xr[s], ga);
            const float gv = __builtin_amdgcn_rcpf(1.f + __builtin_amdgcn_exp2f(-1.4426950408889634f * ga[0]));
            if (lane < 32) ((float*)(ws + WS_G))[tblk * 32 + lane] = gv;
        }
    } else {
        const bool isq = grp < 7; const int u = isq ? grp : grp - 7;
        const float sc = isq ? QSCALE : 1.f;
        char* qb = ws + (isq ? WS_Q : WS_K) + (size_t)tblk * 2048;
#pragma unroll
        for (int g = 0; g < 4; ++g) {
            const int G8 = 8 * u + 4 * hh + g, P = G8 / 7, rr = G8 - P * 7;
            const u32x2 v = {cvtpk(acc[0][0][4 * g] * sc, acc[0][0][4 * g + 1] * sc), cvtpk(acc[0][0][4 * g + 2] * sc, acc[0][0][4 * g + 3] * sc)};
            if (rr == 3) gst16(qb + (size_t)P * 262144 + 1536, h * 131072 + (lane & 31) * 16, (u32x4){v[0], v[1], isq ? 0u : 0x3F803F80u, isq ? 0u : 0x00003F80u});
            else gst8(qb + (size_t)(2 * P + (rr > 3)) * 131072 + (rr & 3) * 512, (lane & 31) * 16 + h * 8, v);
        }
    }
}

__global__ __launch_bounds__(512, 2) void k1(const char* x, const char* wqkv, const char* wg, const char* wu, const char* gw, char* ws) {
    extern __shared__ __attribute__((aligned(16))) char smem[];
    const int b = blockIdx.x, xcd = b & 7, idx = b >> 3;
    const int rowt = (xcd & 1) * 8 + (idx & 7), grp = (xcd >> 1) * 4 + (idx >> 3);
    if (grp < 14) k1_body<false>(x, wqkv, wg, wu, gw, ws, rowt, grp, (lptr)smem);
    else k1_body<true>(x, wqkv, wg, wu, gw, ws, rowt, grp, (lptr)smem);
}

constexpr int K2_SLOT = 45056, K2_XV = 3 * K2_SLOT, K2_Q = K2_XV + 4096, K2_LDS = K2_Q + 8192;
DI float swap_max(float m) { auto rr = __builtin_amdgcn_permlane32_swap(__float_as_uint(m), __float_as_uint(m), false, false); return __builtin_fmaxf(__uint_as_float(rr[0]), __uint_as_float(rr[1])); }
DI float max16(const f32x16& s) {
    float a = __builtin_fmaxf(__builtin_fmaxf(s[0], s[1]), s[2]), b = __builtin_fmaxf(__builtin_fmaxf(s[3], s[4]), s[5]);
    a = __builtin_fmaxf(__builtin_fmaxf(a, s[6]), s[7]); b = __builtin_fmaxf(__builtin_fmaxf(b, s[8]), s[9]);
    a = __builtin_fmaxf(__builtin_fmaxf(a, s[10]), s[11]); b = __builtin_fmaxf(__builtin_fmaxf(b, s[12]), s[13]);
    a = __builtin_fmaxf(__builtin_fmaxf(a, s[14]), s[15]); return __builtin_fmaxf(a, b);
}
DI void set_ref(bf16x8& q1, float m, int h) {
    const unsigned nb = __float_as_uint(-m), hi = nb & 0xFFFF0000u;
    const float r1 = -m - __uint_as_float(hi); const unsigned mid = __float_as_uint(r1) & 0xFFFF0000u;
    const float r2 = r1 - __uint_as_float(mid); const unsigned lo = __float_as_uint(r2) >> 16;
    u32x4 u = __builtin_bit_cast(u32x4, q1);
    if (h) { u[2] = (hi >> 16) | mid; u[3] = lo; }
    q1 = __builtin_bit_cast(bf16x8, u);
}
DI void exp_pack(const f32x16& s, u32x4& p0, u32x4& p1) {
#pragma unroll
    for (int i = 0; i < 4; ++i) p0[i] = cvtpk(__builtin_amdgcn_exp2f(s[2 * i]), __builtin_amdgcn_exp2f(s[2 * i + 1]));
#pragma unroll
    for (int i = 0; i < 4; ++i) p1[i] = cvtpk(__builtin_amdgcn_exp2f(s[8 + 2 * i]), __builtin_amdgcn_exp2f(s[9 + 2 * i]));
}
#define BC8(x) __builtin_bit_cast(bf16x8, x)

template <bool DOWN>
DI void k2_body(const char* wd, char* ws, int head, int qb, int rt, int ct, lptr lds) {
    const int lane = threadIdx.x & 63, w = __builtin_amdgcn_readfirstlane(threadIdx.x >> 6);
    const int kh = w >> 2, h = lane >> 5, nt = w & 1, mt = (w >> 1) & 1;
    const unsigned lds0 = (unsigned)(size_t)lds;
    const int r = lane >> 3, pos = lane & 7;
    const unsigned vlin = lane * 16, vwd = r * 896 + ((pos ^ ((r & 2) << 1)) << 4), v1 = w < 6 ? vlin : vwd;
    const char* kvs = ws + (kh ? WS_V : WS_K) + (size_t)(head * 64 + 32 * mt + nt - kh) * 2048;
    const char* hsrc = ws + WS_H + (size_t)(2 * rt) * 112 * 1024;
    const char* wsrc = wd + ct * 128;
    const char* d0 = hsrc + (size_t)((w / 7) * 112 + (w % 7)) * 1024;
    const char* d1 = w < 6 ? hsrc + (size_t)(112 + w + 1) * 1024 : wsrc + (size_t)(w - 6) * 8 * 896;
    const char* d2 = wsrc + (size_t)(w + 2) * 8 * 896;
    const char* d3 = wsrc + (size_t)(w + 10) * 8 * 896;
    const int inc1 = w < 6 ? 7168 : 112 * 896;
    auto issue = [&](int i, unsigned slot) {
        const char* s = kvs + (size_t)i * 4096; if (i == 0 && kh && nt == 0) s += 2048;
        glds<0>(s, vlin, lds0 + slot + w * 2048); glds<1024>(s, vlin, lds0 + slot + w * 2048);
        if constexpr (DOWN) {
            const unsigned dl = lds0 + slot + 16384 + w * 1024;
            glds<0>(d0 + i * 7168, vlin, dl); glds<0>(d1 + (size_t)i * inc1, v1, dl + 8192); glds<0>(d2 + (size_t)i * (112 * 896), vwd, dl + 16384);
            if (w < 4) glds<0>(d3 + (size_t)i * (112 * 896), vwd, dl + 24576);
        } };
    constexpr int NV = DOWN ? 5 : 2;
    glds<0>(ws + WS_Q + (size_t)(head * 64 + qb * 4) * 2048 + w * 1024, vlin, lds0 + K2_Q + w * 1024);
    issue(0, 0);
    if (w < 4) glds<0>(ws + WS_V + (size_t)(head * 64 + 32 * mt + 31) * 2048 + nt * 1024, vlin, lds0 + K2_XV + w * 1024);
    issue(1, K2_SLOT);
    const int blk = (lane >> 4) & 1, q = (lane & 15) >> 2, p = lane & 3;
    const lptr kv0 = lds + kh * 4096 + vlin, hf0 = lds + 16384 + (mt * 7 + kh * 4) * 1024 + vlin;
    const lptr wd0 = lds + 30720 + kh * 8192 + (4 * h + q) * 128 + (((4 * nt + 2 * blk + (p >> 1)) ^ ((q & 2) << 1)) << 4) + (p & 1) * 8;
    const f32x16 z = {};
    f32x16 o = z, dacc = z, sa, sb; u32x4 p0, p1; float mref;
    auto down = [&](unsigned slot, int f) { dacc = MFMA(ldstr2(wd0 + slot + f * 2048), lds16(hf0 + slot + f * 1024), dacc); };
    wait_bar_v<NV>();
    issue(2, 2 * K2_SLOT);
    const bf16x8 q0 = lds16(lds + K2_Q + (w & 3) * 2048 + vlin); bf16x8 q1 = lds16(lds + K2_Q + (w & 3) * 2048 + 1024 + vlin);
    {
        const bf16x8 k0 = lds16(kv0), k1 = lds16(kv0 + 1024);
        sa = MFMA(k0, q0, z); sa = MFMA(k1, q1, sa);
        mref = swap_max(max16(sa)); set_ref(q1, mref, h);
        sa = MFMA(k0, q0, z); sa = MFMA(k1, q1, sa);
        sb = MFMA(lds16(kv0 + 2048), q0, z); sb = MFMA(lds16(kv0 + 3072), q1, sb);
        if constexpr (DOWN) { down(0, 0); down(0, 1); down(0, 2); if (kh == 0) down(0, 3); }
        exp_pack(sa, p0, p1);
        o = MFMA(lds16(kv0 + 8192 + 2048), BC8(p0), o); o = MFMA(lds16(kv0 + 8192 + 3072), BC8(p1), o);
    }
    unsigned slot = K2_SLOT, nslot = 0;
    for (int i = 1; i < 16; ++i) {
        if (i < 15) wait_bar<NV>(); else wait_bar<0>();
        if (i < 14) issue(i + 2, nslot);
        const lptr kv = kv0 + slot;
        sa = MFMA(lds16(kv), q0, z); sa = MFMA(lds16(kv + 1024), q1, sa);
        if constexpr (DOWN) { down(slot, 0); down(slot, 1); }
        exp_pack(sb, p0, p1);
        o = MFMA(lds16(kv + 8192), BC8(p0), o); o = MFMA(lds16(kv + 8192 + 1024), BC8(p1), o);
        sb = MFMA(lds16(kv + 2048), q0, z); sb = MFMA(lds16(kv + 3072), q1, sb);
        if constexpr (DOWN) { down(slot, 2); if (kh == 0) down(slot, 3); }
        exp_pack(sa, p0, p1);
        o = MFMA(lds16(kv + 8192 + 2048), BC8(p0), o); o = MFMA(lds16(kv + 8192 + 3072), BC8(p1), o);
        nslot = slot; slot = slot == 2 * K2_SLOT ? 0 : slot + K2_SLOT;
    }
    exp_pack(sb, p0, p1);
    o = MFMA(lds16(lds + K2_XV + kh * 2048 + vlin), BC8(p0), o); o = MFMA(lds16(lds + K2_XV + kh * 2048 + 1024 + vlin), BC8(p1), o);
    {
        bool bad = h && !(o[12] <= 1.8446744e19f);
#pragma unroll
        for (int i = 0; i < 16; ++i) bad |= !(__builtin_fabsf(o[i]) <= 3.0e38f);
        if (__builtin_expect(__any(bad), 0)) {
            const char* kg = ws + WS_K + (size_t)(head * 64 + 32 * kh) * 2048 + vlin;
            const char* vg = ws + WS_V + (size_t)(head * 64 + 32 * kh) * 2048 + vlin;
            set_ref(q1, 0.f, h);
            float m = -3.0e38f;
            for (int n = 0; n < 32; ++n) { sa = MFMA(*(const bf16x8*)(kg + n * 2048), q0, z); sa = MFMA(*(const bf16x8*)(kg + n * 2048 + 1024), q1, sa); m = __builtin_fmaxf(m, max16(sa)); }
            mref = swap_max(m); set_ref(q1, mref, h); o = z;
            for (int n = 0; n < 32; ++n) { sa = MFMA(*(const bf16x8*)(kg + n * 2048), q0, z); sa = MFMA(*(const bf16x8*)(kg + n * 2048 + 1024), q1, sa);
                exp_pack(sa, p0, p1);
                o = MFMA(*(const bf16x8*)(vg + n * 2048), BC8(p0), o); o = MFMA(*(const bf16x8*)(vg + n * 2048 + 1024), BC8(p1), o); }
        }
    }
    LAS char* sc = (LAS char*)lds + K2_SLOT + (w & 3) * 8448;
    if (kh) {
#pragma unroll
        for (int g = 0; g < 4; ++g) *(LAS f32x4*)(sc + g * 1024 + vlin) = (f32x4){o[4 * g], o[4 * g + 1], o[4 * g + 2], o[4 * g + 3]};
        *(LAS float*)(sc + 8192 + lane * 4) = mref;
    } else if (DOWN) {
#pragma unroll
        for (int g = 0; g < 4; ++g) *(LAS f32x4*)(sc + 4096 + g * 1024 + vlin) = (f32x4){dacc[4 * g], dacc[4 * g + 1], dacc[4 * g + 2], dacc[4 * g + 3]};
    }
    wait_bar<0>();
    if (!kh) {
        const float m2 = *(LAS float*)(sc + 8192 + lane * 4), ms = __builtin_fmaxf(mref, m2);
        const float f1 = __builtin_amdgcn_exp2f(mref - ms), f2 = __builtin_amdgcn_exp2f(m2 - ms);
#pragma unroll
        for (int g = 0; g < 4; ++g) { const f32x4 v = *(LAS f32x4*)(sc + g * 1024 + vlin);
#pragma unroll
            for (int j = 0; j < 4; ++j) o[4 * g + j] = o[4 * g + j] * f1 + v[j] * f2; }
        auto rr = __builtin_amdgcn_permlane32_swap(__float_as_uint(o[12]), __float_as_uint(o[12]), false, false);
        const float rl = __builtin_amdgcn_rcpf(__uint_as_float(rr[1]));
        u32x4 a, b;
#pragma unroll
        for (int i = 0; i < 4; ++i) { a[i] = cvtpk(o[2 * i] * rl, o[2 * i + 1] * rl); b[i] = cvtpk(o[8 + 2 * i] * rl, o[9 + 2 * i] * rl); }
        if (h) { b[2] = 0; b[3] = 0; }
        char* ob = ws + WS_O + (size_t)((qb * 4 + w) * 16 + head) * 2048;
        gst16(ob, vlin, a); gst16(ob + 1024, vlin, b);
    } else if (DOWN) {
#pragma unroll
        for (int g = 0; g < 4; ++g) { const f32x4 v = *(LAS f32x4*)(sc + 4096 + g * 1024 + vlin);
#pragma unroll
            for (int j = 0; j < 4; ++j) dacc[4 * g + j] += v[j]; }
        u32x4 a, b;
#pragma unroll
        for (int i = 0; i < 4; ++i) { a[i] = cvtpk(dacc[2 * i], dacc[2 * i + 1]); b[i] = cvtpk(dacc[8 + 2 * i], dacc[9 + 2 * i]); }
        char* bb = ws + WS_B + (size_t)((rt * 7 + ct) * 4 + (w & 3)) * 2048;
        gst16(bb, vlin, a); gst16(bb + 1024, vlin, b);
    }
}

__global__ __launch_bounds__(512, 2) void k2(const char* wd, char* ws) {
    extern __shared__ __attribute__((aligned(16))) char smem[];
    const int b = blockIdx.x, xcd = b & 7, idx = b >> 3;
    const int head = 2 * xcd + (idx >> 4), qb = idx & 15;
    if (idx < 28) k2_body<true>(wd, ws, head, qb, 4 * xcd + idx / 7, idx % 7, (lptr)smem);
    else k2_body<false>(wd, ws, head, qb, 0, 0, (lptr)smem);
}

constexpr int K3_WO = 65536, K3_SC = K3_WO + 452 * 128, K3_LDS = K3_SC + 16384 + 8192;
__global__ __launch_bounds__(512, 2) void k3(const char* wo, const char* ws, char* out) {
    extern __shared__ __attribute__((aligned(16))) char smem[];
    const lptr lds = (lptr)smem;
    const int b = blockIdx.x, xcd = b & 7, idx = b >> 3, rt = 4 * xcd + idx / 7, ct = idx % 7;
    const int lane = threadIdx.x & 63, w = __builtin_amdgcn_readfirstlane(threadIdx.x >> 6);
    const int kh = w >> 2, h = lane >> 5, nt = w & 1, mt = (w >> 1) & 1;
    const unsigned lds0 = (unsigned)(size_t)lds;
    const int r = lane >> 3, pos = lane & 7;
    const unsigned vlin = lane * 16, vwd = r * 896 + ((pos ^ ((r & 2) << 1)) << 4);
    const char* os = ws + WS_O + (size_t)(2 * rt) * 32768 + w * 8192;
    static_for<2>([&](auto I) { glds<0>(os + I * 4096, vlin, lds0 + w * 8192 + I * 4096); glds<1024>(os + I * 4096, vlin, lds0 + w * 8192 + I * 4096);
        glds<2048>(os + I * 4096, vlin, lds0 + w * 8192 + I * 4096); glds<3072>(os + I * 4096, vlin, lds0 + w * 8192 + I * 4096); });
    const char* wsrc = wo + ct * 128 + (size_t)w * 56 * 896;
    static_for<7>([&](auto I) { glds<0>(wsrc + I * 8 * 896, vwd, lds0 + K3_WO + (w * 7 + I) * 1024); });
    if (threadIdx.x < 32) *(LAS u32x4*)((LAS char*)lds + K3_WO + 448 * 128 + threadIdx.x * 16) = (u32x4){0, 0, 0, 0};
    const char* bb = ws + WS_B + (size_t)((rt * 7 + ct) * 4 + (w & 3)) * 2048;
    const u32x4 bv = *(const u32x4*)(bb + kh * 1024 + vlin);
    const float g = ((const float*)(ws + WS_G))[rt * 64 + mt * 32 + (lane & 31)];
    const int blk = (lane >> 4) & 1, q = (lane & 15) >> 2, p = lane & 3;
    const lptr of0 = lds + mt * 32768 + kh * 16384 + vlin;
    const lptr wo0 = lds + K3_WO + (kh * 224 + 4 * h + q) * 128 + (((4 * nt + 2 * blk + (p >> 1)) ^ ((q & 2) << 1)) << 4) + (p & 1) * 8;
    wait_bar<0>();
    f32x16 acc = {};
#pragma unroll
    for (int s = 0; s < 16; ++s) acc = MFMA(ldstr2(wo0 + ((s >> 1) * 28 + (s & 1) * 16) * 128), lds16(of0 + s * 1024), acc);
    LAS char* sc = (LAS char*)lds + K3_SC + (w & 3) * 4096;
#pragma unroll
    for (int i = 0; i < 2; ++i) { const int o = (kh ? 0 : 8) + 4 * i;
        *(LAS f32x4*)(sc + kh * 2048 + i * 1024 + vlin) = kh ? (f32x4){acc[4 * i], acc[4 * i + 1], acc[4 * i + 2], acc[4 * i + 3]} : (f32x4){acc[8 + 4 * i], acc[9 + 4 * i], acc[10 + 4 * i], acc[11 + 4 * i]}; (void)o; }
    wait_bar<0>();
    LAS char* ot = (LAS char*)lds + K3_SC + 16384;
    const int tok = mt * 32 + (lane & 31);
#pragma unroll
    for (int i = 0; i < 2; ++i) {
        const f32x4 v = *(LAS f32x4*)(sc + (kh ^ 1) * 2048 + i * 1024 + vlin);
        float a[4], bq[4];
#pragma unroll
        for (int j = 0; j < 4; ++j) a[j] = (kh ? acc[8 + 4 * i + j] : acc[4 * i + j]) + v[j];
        bq[0] = __uint_as_float(bv[2 * i] << 16); bq[1] = __uint_as_float(bv[2 * i] & 0xFFFF0000u); bq[2] = __uint_as_float(bv[2 * i + 1] << 16); bq[3] = __uint_as_float(bv[2 * i + 1] & 0xFFFF0000u);
#pragma unroll
        for (int j = 0; j < 4; ++j) a[j] = g * a[j] + (1.f - g) * bq[j];
        const int ch = nt * 4 + kh * 2 + i;
        *(LAS u32x2*)(ot + tok * 128 + ((ch ^ (tok & 7)) << 4) + h * 8) = (u32x2){cvtpk(a[0], a[1]), cvtpk(a[2], a[3])};
    }
    wait_bar<0>();
    const int row = threadIdx.x >> 3;
    const u32x4 ov = *(LAS u32x4*)(ot + row * 128 + ((pos ^ (row & 7)) << 4));
    *(u32x4*)(out + (size_t)(rt * 64 + row) * 896 + ct * 128 + pos * 16) = ov;
}

extern "C" void kernel_launch(void* const* d_in, const int* in_sizes, int n_in, void* d_out, int out_size, void* d_ws, size_t ws_size, hipStream_t stream) {
    (void)in_sizes; (void)n_in; (void)out_size; (void)ws_size;
    (void)hipFuncSetAttribute((const void*)k1, hipFuncAttributeMaxDynamicSharedMemorySize, 163840);
    (void)hipFuncSetAttribute((const void*)k2, hipFuncAttributeMaxDynamicSharedMemorySize, K2_LDS);
    (void)hipFuncSetAttribute((const void*)k3, hipFuncAttributeMaxDynamicSharedMemorySize, K3_LDS);
    k1<<<256, 512, 163840, stream>>>((const char*)d_in[0], (const char*)d_in[1], (const char*)d_in[4], (const char*)d_in[5], (const char*)d_in[7], (char*)d_ws);
    k2<<<256, 512, K2_LDS, stream>>>((const char*)d_in[6], (char*)d_ws);
    k3<<<224, 512, K3_LDS, stream>>>((const char*)d_in[2], (const char*)d_ws, (char*)d_out);
}
```

```cpp
#include <hip/hip_runtime.h>
#include <hip/hip_bf16.h>
#include <utility>

typedef short bf16x8 __attribute__((ext_vector_type(8)));
typedef short s16x4 __attribute__((ext_vector_type(4)));
typedef float f32x16 __attribute__((ext_vector_type(16)));
typedef float f32x4 __attribute__((ext_vector_type(4)));
typedef unsigned u32x4 __attribute__((ext_vector_type(4)));
typedef unsigned u32x2 __attribute__((ext_vector_type(2)));
#define LAS __attribute__((address_space(3)))
#define GAS __attribute__((address_space(1)))
#define DI __device__ __forceinline__
#define MFMA(a, b, c) __builtin_amdgcn_mfma_f32_32x32x16_bf16((a), (b), (c), 0, 0, 0)
#define SBAR() __builtin_amdgcn_sched_barrier(0)
#define PIN(x) asm volatile("" : "+v"(x))
#define PINS(x) asm volatile("" : "+s"(x))

constexpr size_t WS_Q = 0, WS_K = 2u << 20, WS_V = 4u << 20, WS_O = 6u << 20, WS_H = 8u << 20, WS_G = 15u << 20, WS_F = WS_G + 65536;
constexpr int F_QKV = 32, F_HA = F_QKV + 256, F_HB = F_HA + 256, F_O = F_HB + 256;
constexpr float QSCALE = 0.1889822365046136f * 1.4426950408889634f;


template <int... I, class F> DI void static_for_impl(std::integer_sequence<int, I...>, F&& f) { (f(std::integral_constant<int, I>{}), ...); }
template <int N, class F> DI void static_for(F&& f) { static_for_impl(std::make_integer_sequence<int, N>{}, f); }

typedef __bf16 bf16x2_t __attribute__((ext_vector_type(2)));
typedef float f32x2 __attribute__((ext_vector_type(2)));
DI unsigned cvtpk(float lo, float hi) { return __builtin_bit_cast(unsigned, __builtin_convertvector((f32x2){lo, hi}, bf16x2_t)); }
template <int OFF> DI void glds(const char* sbase, unsigned voff, unsigned lds) {
    asm volatile("s_mov_b32 m0, %2\n\ts_nop 0\n\tglobal_load_lds_dwordx4 %0, %1 offset:%3" ::"v"(voff), "s"(sbase), "s"(lds), "i"(OFF) : "memory");
}
template <int OFF> DI void glds_sc1(const char* sbase, unsigned voff, unsigned lds) {
    asm volatile("s_mov_b32 m0, %2\n\ts_nop 0\n\tglobal_load_lds_dwordx4 %0, %1 offset:%3 sc1" ::"v"(voff), "s"(sbase), "s"(lds), "i"(OFF) : "memory");
}
DI u32x4 make_srd(const char* p) { const size_t a = (size_t)p; return (u32x4){(unsigned)a, (unsigned)(a >> 32) & 0xFFFFu, 0xFFFFFFFFu, 0x00020000u}; }
template <int ADD> DI void set_m0(unsigned base) { if constexpr (ADD == 0) asm volatile("s_mov_b32 m0, %0" ::"s"(base)); else asm volatile("s_add_u32 m0, %0, %1" ::"s"(base), "i"(ADD) : "scc"); }
template <int OFF, bool SC1> DI void blds(u32x4 srd, unsigned voff, unsigned soff) {
    if constexpr (SC1) asm volatile("buffer_load_dwordx4 %0, %1, %2 offen offset:%3 sc1 lds" ::"v"(voff), "s"(srd), "s"(soff), "i"(OFF) : "memory");
    else asm volatile("buffer_load_dwordx4 %0, %1, %2 offen offset:%3 lds" ::"v"(voff), "s"(srd), "s"(soff), "i"(OFF) : "memory");
}
typedef unsigned long long u64;
DI void poll_issue(u64& v, const char* sbase, unsigned voff, u64 tag) { v = tag - 1; asm volatile("global_load_dwordx2 %0, %1, %2 sc1" : "+v"(v) : "v"(voff), "s"(sbase) : "memory"); }
template <int N, int SLEEP = 4> DI void poll_check(u64& v, const char* sbase, unsigned voff, u64 tag) {
    asm volatile("s_waitcnt vmcnt(%1)" : "+v"(v) : "i"(N) : "memory");
#pragma unroll 1
    for (unsigned spins = 0; !__all(v == tag) && spins < (1u << 21); ++spins) {
        if constexpr (SLEEP > 0) __builtin_amdgcn_s_sleep(SLEEP);
        asm volatile("global_load_dwordx2 %0, %1, %2 sc1\n\ts_waitcnt vmcnt(0)" : "=&v"(v) : "v"(voff), "s"(sbase) : "memory");
    }
}
DI void flag_set(char* ws, int word, u64 tag) { __hip_atomic_store((GAS u64*)(ws + WS_F) + word, tag, __ATOMIC_RELAXED, __HIP_MEMORY_SCOPE_AGENT); }
DI unsigned lds_count(LAS char* p) { return __hip_atomic_fetch_add((LAS unsigned*)p, 1u, __ATOMIC_RELAXED, __HIP_MEMORY_SCOPE_WORKGROUP); }
DI void gst16(char* sbase, unsigned voff, u32x4 v) { asm volatile("global_store_dwordx4 %0, %1, %2 sc1\n\ts_nop 1" ::"v"(voff), "v"(v), "s"(sbase) : "memory"); }
DI void gst8(char* sbase, unsigned voff, u32x2 v) { asm volatile("global_store_dwordx2 %0, %1, %2 sc1" ::"v"(voff), "v"(v), "s"(sbase) : "memory"); }
DI void gld4_sc1(u32x4* d, const char* sbase, unsigned voff) {
    asm volatile("global_load_dwordx4 %0, %4, %5 sc1\n\tglobal_load_dwordx4 %1, %4, %5 offset:1024 sc1\n\tglobal_load_dwordx4 %2, %4, %5 offset:2048 sc1\n\tglobal_load_dwordx4 %3, %4, %5 offset:3072 sc1"
                 : "=&v"(d[0]), "=&v"(d[1]), "=&v"(d[2]), "=&v"(d[3]) : "v"(voff), "s"(sbase) : "memory");
}
template <int N> DI void wait4(u32x4* d) { asm volatile("s_waitcnt vmcnt(%4)" : "+v"(d[0]), "+v"(d[1]), "+v"(d[2]), "+v"(d[3]) : "i"(N) : "memory"); }
template <int N> DI void wait_bar() {
    asm volatile("" ::: "memory"); __builtin_amdgcn_s_waitcnt((N & 15) | ((N >> 4) << 14) | 0x70); __builtin_amdgcn_s_barrier(); asm volatile("" ::: "memory"); }
template <int N> DI void wait_bar_v() { asm volatile("s_waitcnt vmcnt(%0)\n\ts_barrier" ::"i"(N) : "memory"); }
typedef LAS const char* lptr;
DI bf16x8 lds16(lptr p) { return *(const LAS bf16x8*)p; }
typedef short v4i16_t __attribute__((ext_vector_type(4)));
DI bf16x8 ldstr(lptr p) {
    s16x4 lo = __builtin_bit_cast(s16x4, __builtin_amdgcn_ds_read_tr16_b64_v4i16((LAS v4i16_t*)p));
    s16x4 hi = __builtin_bit_cast(s16x4, __builtin_amdgcn_ds_read_tr16_b64_v4i16((LAS v4i16_t*)(p + 512)));
    return __builtin_shufflevector(lo, hi, 0, 1, 2, 3, 4, 5, 6, 7);
}
DI bf16x8 ldstr2(lptr p) {
    s16x4 lo = __builtin_bit_cast(s16x4, __builtin_amdgcn_ds_read_tr16_b64_v4i16((LAS v4i16_t*)p));
    s16x4 hi = __builtin_bit_cast(s16x4, __builtin_amdgcn_ds_read_tr16_b64_v4i16((LAS v4i16_t*)(p + 1024)));
    return __builtin_shufflevector(lo, hi, 0, 1, 2, 3, 4, 5, 6, 7);
}

constexpr int L_SC = 86016, L_CNT = L_SC + 9216, L_WO = 96256, L_KV = 106496, L_Q = 155648, L_END = 163840;

struct Role { int rowt, grp, head, qb, rt, ct; };

DI unsigned p2_flag_off(int head, int w, int lane) {
    const int u0 = (28 * head) >> 6, u1 = (28 * head + 27) >> 6, u = (lane & 16) ? u1 : u0;
    const int g = w < 4 ? ((lane & 32) ? u : (u == 6 ? 15 : 7 + u)) : ((lane & 32) ? 15 : 13 + u / 3);
    return (F_QKV + g * 16 + (lane & 15)) * 8;
}
template <int S> DI void store_v(char* ws, int tblk, int c, int h, const f32x16& a) {
    const int head = c / 28, d = c - head * 28;
    gst16(ws + WS_V + (size_t)tblk * 2048 + S * 1024, head * 131072 + (h * 32 + d + 4 * (head & 1)) * 16,
          (u32x4){cvtpk(a[8 * S], a[8 * S + 1]), cvtpk(a[8 * S + 2], a[8 * S + 3]), cvtpk(a[8 * S + 4], a[8 * S + 5]), cvtpk(a[8 * S + 6], a[8 * S + 7])});
}
DI unsigned qk_off(bool isq, int tblk, int G8) {
    const int P = G8 / 7, rr = G8 - P * 7;
    return (isq ? WS_Q : WS_K) + tblk * 2048 + (rr == 3 ? P * 262144 + 1537 : (2 * P + (rr >> 2)) * 131072 + (rr & 3) * 512);
}
DI void store_qk(char* ws, bool isq, unsigned off, int lane, float a0, float a1, float a2, float a3) {
    const float sc = isq ? QSCALE : 1.f; const int h = lane >> 5;
    const u32x2 v = {cvtpk(a0 * sc, a1 * sc), cvtpk(a2 * sc, a3 * sc)};
    if (__builtin_expect(off & 1, 0)) gst16(ws + (off - 1), h * 131072 + (lane & 31) * 16, (u32x4){v[0], v[1], isq ? 0u : 0x3F803F80u, isq ? 0u : 0x00003F80u});
    else gst8(ws + off, (lane & 31) * 16 + h * 8, v);
}
DI void p2_first_tiles(char* ws, const Role& R, int w, unsigned vlin, unsigned lds0) {
    const char* kvs = ws + (w < 4 ? WS_K : WS_V) + (size_t)(R.head * 64 + 32 * ((w >> 1) & 1) + (w & 1)) * 2048;
    if (w < 4) { const char* qs = ws + WS_Q + (size_t)(R.head * 64 + R.qb * 4 + w) * 2048; glds_sc1<0>(qs, vlin, lds0 + L_Q + w * 2048); glds_sc1<1024>(qs, vlin, lds0 + L_Q + w * 2048); }
    glds_sc1<0>(kvs, vlin, lds0 + L_KV + 16384 + w * 2048); glds_sc1<1024>(kvs, vlin, lds0 + L_KV + 16384 + w * 2048);
    glds_sc1<0>(kvs + 4096, vlin, lds0 + L_KV + 32768 + w * 2048); glds_sc1<1024>(kvs + 4096, vlin, lds0 + L_KV + 32768 + w * 2048);
}
DI u64 uniform64(u64 v) { return ((u64)(unsigned)__builtin_amdgcn_readfirstlane((unsigned)(v >> 32)) << 32) | (unsigned)__builtin_amdgcn_readfirstlane((unsigned)v); }

DI unsigned touch(const char* a) { unsigned v; asm volatile("global_load_dword %0, %1, off sc1" : "=&v"(v) : "v"(a) : "memory"); return v; }
constexpr int CODE_LINES = 14;
DI unsigned warm_code(const char* code, int w, int lane) { unsigned v = 0; if (w == 7 && lane < CODE_LINES) v = touch(code + ((int)(blockIdx.x >> 3) * CODE_LINES + lane) * 128); return v; }
constexpr int P1_PRE = 8, P1_DBL = 4;
constexpr int p1_piece0(int g) { return g <= 17 ? 2 * g : 2 * g - 1; }
constexpr int p1_gran(int pc) { return pc < 35 ? pc / 2 : (pc + 1) / 2; }
constexpr int p1_first(int g) { return g < 14 ? 4 * (g / 2) : g < 18 ? 28 + 4 * (g - 14) : 42 + 4 * (g - 18); }
constexpr int p1_lastuse(int g) { return g == 17 ? 41 : p1_first(g) + 3; }
constexpr int p1_pub(bool vt) { return vt ? 48 : 52; }
constexpr bool p1_bar(int ms) { return (ms % 4 == 2 && ms <= 26) || ms == 34 || ms == 42; }
constexpr int p1_prevbar(int ms) { for (int i = ms; i >= 0; --i) if (p1_bar(i)) return i; return 0; }
constexpr int p1_nextbar(int ms);
constexpr int p1_prio(int ms) { if (ms < 26) return 0; const int a = ms >= 52 ? 52 : p1_prevbar(ms), nb = p1_nextbar(ms), b = ms >= 52 ? 70 : nb > 52 ? 52 : nb; return 3 - (ms - a) * 4 / (b - a); }
constexpr int p1_nextbar(int ms) { for (int i = ms + 1; i < 70; ++i) if (p1_bar(i)) return i; return 1000; }
constexpr int p1_need(int ms, bool vt) { const int nb = ms == 42 ? p1_pub(vt) : p1_nextbar(ms); int g = 0; while (g < 24 && p1_first(g + 1) - 2 < nb) ++g; return p1_piece0(g + 1) - 1; }
constexpr int p1_limit(int ms) { int b = -1; for (int i = 0; i <= ms; ++i) if (p1_bar(i)) b = i;
    int g = 9; while (g < 24 && p1_lastuse(g + 1 - 10) < b) ++g; return p1_piece0(g + 1) - 1; }
constexpr int p1_piece_at(int gp) {
    int next = P1_PRE;
    for (int j = 0; j <= gp; ++j) { const bool go = ((j & 1) || j / 2 < P1_DBL) && next <= p1_limit(j / 2) && next < 49; if (j == gp) return go ? next : -1; if (go) ++next; }
    return -1;
}
constexpr int p1_op0(int gap) { int g = gap - 58; if (gap > 86) g -= gap >= 90 ? 4 : gap - 86; return g <= 0 ? 0 : (g * 136 / 60 > 136 ? 136 : g * 136 / 60); }
constexpr int p1_stores_in(int gap, bool vt) { const int a = p1_op0(gap), b = p1_op0(gap + 1);
    return vt ? (gap == 60 || gap == 64 || gap == 68 || gap == 72 || gap == 86 || gap == 88) : (a <= 67 && 67 < b) + (a <= 135 && 135 < b) + (gap >= 86 && gap <= 89); }
constexpr int p1_wait(int bms, bool vt) {
    const int last = p1_need(bms, vt); int cnt = 0; bool seen = false;
    for (int g = 0; g < P1_PRE; ++g) { if (seen) ++cnt; if (g == last) seen = true; }
    if (seen) ++cnt;
    for (int ms = 0; ms < 70; ++ms) {
        if (ms == bms || bms < 0) return seen ? cnt : -1000;
        for (int sl = 0; sl < 2; ++sl) {
            const int pc = p1_piece_at(2 * ms + sl); if (pc >= 0) { if (seen) ++cnt; if (pc == last) { seen = true; cnt = 0; } }
            const int gp = 2 * ms + sl; if (p1_stores_in(gp, vt)) { if (bms == p1_pub(vt) && gp >= 86 && gp <= 89) cnt = 0; else if (seen) cnt += p1_stores_in(gp, vt); }
        }
    }
    return -1000;
}

template <bool VT>
DI u64 phase1_main(const char* x, const char* wqkv, const char* wg, const char* wu, const char* code, char* ws, const Role& R, lptr lds) {
    const int lane = threadIdx.x & 63, w = __builtin_amdgcn_readfirstlane(threadIdx.x >> 6);
    const int tb = w & 3, hh = w >> 2, h = lane >> 5, grp = R.grp;
    const unsigned lds0 = (unsigned)(size_t)lds;
    const int r = lane >> 3, pos = lane & 7;
    const unsigned vx0 = r * 896 + ((pos ^ (r >> 1)) << 4), vx1 = (8 + r) * 896 + ((pos ^ (4 | (r >> 1))) << 4);
    const unsigned vw = r * 3584 + ((pos ^ ((hh << 2) | (r & 2))) << 4), vc = r * 2688 + ((pos ^ ((r & 2) << 1)) << 4);
    const char* wbp = (hh ? wu : wg) + (13 + grp) * 128 + (size_t)(16 * tb) * 3584;
    const u32x4 rx = make_srd(x + (size_t)(R.rowt * 128 + 16 * w) * 896), rb = make_srd(wbp);
    const u32x4 ra = make_srd(VT ? wqkv + 1792 + (3 * (grp - 13) + hh) * 128 + (size_t)(16 * tb) * 2688 : wbp - 13 * 128);
    const u32x4 rc = make_srd(VT ? wqkv + 1792 + (3 * (grp - 13) + 2) * 128 : wqkv + grp * 128);
    const unsigned ldw = lds0 + w * 2048;
    const int rot = (grp + 2 * R.rowt) % 7;
    auto kb = [&](int j) { const int v = j + rot; return v >= 7 ? v - 7 : v; };
    auto issue2 = [&](auto PC_, auto PART_) { constexpr int PC = PC_, G = p1_gran(PC), E = PC - p1_piece0(G); constexpr int sl = (G % 10) * 16384 + E * 1024;
        if constexpr (PART_ == 0) { if constexpr (G == 17) set_m0<sl>(lds0 + w * 1024); else set_m0<sl>(ldw); }
        else if constexpr (G < 14 && (G & 1) == 0) blds<0, false>(rx, E ? vx1 : vx0, kb(G / 2) * 128);
        else if constexpr (G < 14) blds<0, false>(ra, VT ? vc : vw, (kb(G / 2) * 64 + E * 8) * (VT ? 2688 : 3584));
        else if constexpr (G < 17) blds<0, false>(rc, vc, (kb(2 * (G - 14) + hh) * 64 + 16 * tb + 8 * E) * 2688);
        else if constexpr (G == 17) blds<0, false>(rc, vc, (kb(6) * 64 + 8 * w) * 2688);
        else blds<0, false>(rb, vw, (kb(G - 18) * 64 + E * 8) * 3584); };
    auto issue = [&](auto PC_) { issue2(PC_, std::integral_constant<int, 0>{}); asm volatile("s_nop 0"); issue2(PC_, std::integral_constant<int, 1>{}); };
    u64 tag;
    static_for<4>([&](auto I) { issue(I); });
    __builtin_amdgcn_s_barrier();
    static_for<P1_PRE - 4>([&](auto I) { issue(std::integral_constant<int, 4 + I>{}); });
    poll_issue(tag, ws + WS_F, 0, 1);
    unsigned tch2 = 0;
    const int t = 32 * tb + (lane & 31), gx = (t >> 1) & 7;
    const int blk = (lane >> 4) & 1, q = (lane & 15) >> 2, p = lane & 3;
    unsigned xa[4], wad[2][3], cad;
#pragma unroll
    for (int s = 0; s < 4; ++s) xa[s] = lds0 + t * 128 + (((2 * s + h) ^ gx) << 4);
    auto set_wad = [&](bool v) {
#pragma unroll
        for (int a = 0; a < 2; ++a)
#pragma unroll
            for (int wi = 0; wi < 3; ++wi) { wad[a][wi] = lds0 + wi * 65536 + (8 * h + q) * 128 + (p & 1) * 8 + (v ? hh * 8192 + (((4 * a + 2 * blk + (p >> 1)) ^ ((q & 2) << 1)) << 4)
                                                                                                          : blk * 8192 + (((2 * (2 * hh + a) + (p >> 1)) ^ ((blk << 2) | (q & 2))) << 4)); PIN(wad[a][wi]); } };
    set_wad(VT);
    bf16x8 xr[28], wf[3][2];
    f32x16 acc[2][2], accq;
    u32x4 pk;
    const int tblk = R.rowt * 4 + tb;
    auto readf = [&](auto MS_) { constexpr int MS = MS_, B = MS % 3;
        if constexpr (MS < 28) { constexpr int ox = ((2 * (MS / 4)) % 10) * 16384; unsigned a = xa[MS & 3] + (ox & ~65535); if constexpr (ox >= 65536) PIN(a);
            xr[MS] = lds16((lptr)(size_t)a + (ox & 65535)); }
        if constexpr (VT && MS == 42) set_wad(false);
        if constexpr (MS < 28 || MS >= 42) { constexpr int o = MS < 28 ? ((2 * (MS / 4) + 1) % 10) * 16384 + (MS & 3) * 2048 : ((18 + (MS - 42) / 4) % 10) * 16384 + ((MS - 42) & 3) * 2048;
            wf[B][0] = ldstr((lptr)(size_t)wad[0][o >> 16] + (o & 65535)); wf[B][1] = ldstr((lptr)(size_t)wad[1][o >> 16] + (o & 65535)); }
        else { constexpr int o = (MS - 28) * 4096;
            if constexpr (MS == 28) { cad = lds0 + 65536 + (8 * h + q) * 128 + (((4 * hh + 2 * blk + (p >> 1)) ^ ((q & 2) << 1)) << 4) + (p & 1) * 8; PIN(cad); }
            wf[B][0] = ldstr((lptr)(size_t)cad + o); wf[B][1] = ldstr((lptr)(size_t)cad + o + 2048); } };
    float tmp[4];
    auto gop = [&](auto PP_, auto OP_) { constexpr int PP = PP_, OP = OP_, GR = OP / 34, K = OP % 34, A = GR >> 1, J = (GR & 1) * 4 + (K & 3);
        f32x16& ac = acc[PP][A];
        if constexpr (K < 4) tmp[K & 3] = ac[J] * ac[J];
        else if constexpr (K < 8) tmp[K & 3] = __builtin_fmaf(tmp[K & 3], -0.1029432397f, -2.302208198f);
        else if constexpr (K < 12) tmp[K & 3] = tmp[K & 3] * ac[J];
        else if constexpr (K < 16) ac[8 + J] = ac[8 + J] * ac[J];
        else if constexpr (K < 20) tmp[K & 3] = __builtin_amdgcn_exp2f(tmp[K & 3]);
        else if constexpr (K < 24) tmp[K & 3] = tmp[K & 3] + 1.f;
        else if constexpr (K < 28) tmp[K & 3] = __builtin_amdgcn_rcpf(tmp[K & 3]);
        else if constexpr (K < 32) tmp[K & 3] = tmp[K & 3] * ac[8 + J];
        else pk[(GR & 1) * 2 + (K & 1)] = cvtpk(tmp[(K & 1) * 2], tmp[(K & 1) * 2 + 1]);
        if constexpr (K >= 12 && K < 16) { float v = ac[8 + J]; PIN(v); ac[8 + J] = v; } else if constexpr (K < 32) PIN(tmp[K & 3]); else { unsigned v = pk[(GR & 1) * 2 + (K & 1)]; PIN(v); pk[(GR & 1) * 2 + (K & 1)] = v; }
        if constexpr (OP == 67 || OP == 135) gst16(ws + WS_H + (size_t)(tblk * 112 + (13 * PP + grp) * 4 + 2 * hh + A) * 1024, lane * 16, pk); };
    unsigned qko[4];
#pragma unroll
    for (int g = 0; g < 4; ++g) { qko[g] = qk_off(grp < 7, tblk, 8 * (grp < 7 ? grp : grp - 7) + 4 * hh + g); if (!VT) asm volatile("" : "+s"(qko[g])); }
    auto gap = [&](auto GP_) { constexpr int GP = GP_;
        if constexpr (VT) {
            if constexpr (GP >= 60 && GP <= 72 && GP % 4 == 0) store_v<(GP / 4 - 15) & 1>(ws, tblk, (3 * (grp - 13) + hh) * 64 + ((GP / 4 - 15) >> 1) * 32 + (lane & 31), h, acc[0][(GP / 4 - 15) >> 1]);
            if constexpr (GP == 86 || GP == 88) store_v<(GP - 86) / 2>(ws, tblk, (3 * (grp - 13) + 2) * 64 + hh * 32 + (lane & 31), h, accq);
        } else {
            if constexpr (GP >= 86 && GP <= 89) { constexpr int G = GP - 86; store_qk(ws, grp < 7, qko[G], lane, accq[4 * G], accq[4 * G + 1], accq[4 * G + 2], accq[4 * G + 3]); }
            if constexpr (p1_op0(GP + 1) > p1_op0(GP)) static_for<p1_op0(GP + 1) - p1_op0(GP)>([&](auto I) { gop(std::integral_constant<int, 0>{}, std::integral_constant<int, p1_op0(GP) + I>{}); });
        } };
#define MFO(W, X, C) (VT ? MFMA(X, W, C) : MFMA(W, X, C))
    wait_bar_v<p1_wait(-1, VT)>();
    u64 fl; unsigned foff;
    readf(std::integral_constant<int, 0>{}); readf(std::integral_constant<int, 1>{});
    static_for<70>([&](auto MS_) { constexpr int MS = MS_, B = MS % 3;
        SBAR();
        if constexpr (p1_bar(MS) || MS == p1_pub(VT)) {
            static_assert(p1_wait(MS, VT) >= 0, "phase 1 schedule");
            if constexpr (MS == p1_pub(VT) && MS < 50) wait_bar<p1_wait(MS, VT)>(); else wait_bar_v<p1_wait(MS, VT)>();
            if constexpr (MS == 42) { if (threadIdx.x == 0) *(LAS unsigned*)((LAS char*)lds + L_CNT + 16) = 0; }
            if constexpr (MS == p1_pub(VT)) { if (threadIdx.x == 0) flag_set(ws, F_QKV + grp * 16 + R.rowt, tag); }
        }
        static_assert(p1_op0(130) == 136, "phase 1 schedule");
        if constexpr (MS == 67 && !VT) { asm volatile("s_waitcnt vmcnt(1)" ::: "memory");
            if (lane == 0 && lds_count((LAS char*)lds + L_CNT + 16) == 7u) flag_set(ws, F_HA + R.rowt * 16 + grp, tag); }
        if constexpr (MS == 65) { int l2 = threadIdx.x; PIN(l2); foff = p2_flag_off(R.head, w, l2 & 63); poll_issue(fl, ws + WS_F, foff, tag); }
        if constexpr (MS == 0) tch2 = warm_code(code, w, lane);
        if constexpr (MS == 11) { PIN(tch2); PIN(tag); tag = uniform64(tag) + 1; }
        if constexpr (MS > 0 && p1_prio(MS) != p1_prio(MS - 1)) __builtin_amdgcn_s_setprio(p1_prio(MS));
        if constexpr (MS + 2 < 70) readf(std::integral_constant<int, MS + 2>{});
        SBAR();
        if constexpr (p1_piece_at(2 * MS) >= 0) { issue(std::integral_constant<int, p1_piece_at(2 * MS)>{}); SBAR(); }
        if constexpr (p1_piece_at(2 * MS + 1) >= 0) { issue2(std::integral_constant<int, p1_piece_at(2 * MS + 1)>{}, std::integral_constant<int, 0>{}); SBAR(); }
        const f32x16 z = {};
        if constexpr (MS < 28) acc[0][0] = MFO(wf[B][0], xr[MS], MS == 0 ? z : acc[0][0]);
        else if constexpr (MS < 42) accq = MFO(wf[B][0], xr[2 * (MS - 28)], MS == 28 ? z : accq);
        else acc[1][0] = MFMA(wf[B][0], xr[MS - 42], MS == 42 ? z : acc[1][0]);
        SBAR();
        gap(std::integral_constant<int, 2 * MS>{}); SBAR();
        if constexpr (p1_piece_at(2 * MS + 1) >= 0) { issue2(std::integral_constant<int, p1_piece_at(2 * MS + 1)>{}, std::integral_constant<int, 1>{}); SBAR(); }
        if constexpr (MS < 28) acc[0][1] = MFO(wf[B][1], xr[MS], MS == 0 ? z : acc[0][1]);
        else if constexpr (MS < 42) accq = MFO(wf[B][1], xr[2 * (MS - 28) + 1], accq);
        else acc[1][1] = MFMA(wf[B][1], xr[MS - 42], MS == 42 ? z : acc[1][1]);
        SBAR();
        gap(std::integral_constant<int, 2 * MS + 1>{});
    });
    SBAR();
    poll_check<0>(fl, ws + WS_F, foff, tag);
    p2_first_tiles(ws, R, w, lane * 16, lds0);
    SBAR();
    static_for<136>([&](auto I) { gop(std::integral_constant<int, 1>{}, I); });
    wait_bar<2>();
    return tag;
}

constexpr int pc_piece_at(int ms) { int next = 12;
    for (int j = 0; j <= ms; ++j) { const int m = j % 4 >= 2 ? j / 4 + 1 : j / 4, lim = m < 2 ? 15 : 4 * (m - 2) + 19; const bool go = next <= lim && next < 28; if (j == ms) return go ? next : -1; if (go) ++next; }
    return -1; }
constexpr int pc_wait(int m) { const int last = 4 * m + 3; int cnt = 0; bool seen = false;
    for (int g = 0; g < 12; ++g) { if (seen) ++cnt; if (g == last) seen = true; }
    if (m == 0) return cnt;
    for (int ms = 0; ms < 28; ++ms) { if (ms % 4 == 2 && ms / 4 + 1 == m) return seen ? cnt : -1000; const int pc = pc_piece_at(ms); if (pc >= 0) { if (seen) ++cnt; if (pc == last) seen = true; } }
    return -1000; }

template <bool QK>
DI u64 phase1_last(const char* x, const char* wqkv, const char* gw, const char* wd, const char* wo, const char* code, char* ws, const Role& R, lptr lds) {
    const int lane = threadIdx.x & 63, w = __builtin_amdgcn_readfirstlane(threadIdx.x >> 6);
    const int tb = w & 3, h = lane >> 5;
    const unsigned lds0 = (unsigned)(size_t)lds;
    const int r = lane >> 3, pos = lane & 7;
    const unsigned vx0 = r * 896 + ((pos ^ (r >> 1)) << 4), vx1 = (8 + r) * 896 + ((pos ^ (4 | (r >> 1))) << 4), vc = r * 2688 + ((pos ^ ((r & 2) << 1)) << 4);
    const char* xb = x + (size_t)(R.rowt * 128 + 16 * w) * 896;
    const char* wa = wqkv + (QK ? 13 : 20) * 128 + (size_t)(16 * tb) * 2688;
    const unsigned ldw = lds0 + w * 2048;
    auto issue = [&](auto PC_) { constexpr int PC = PC_, J = PC / 4, E = PC % 4, SL = ((PC / 2) % 8) * 16384 + (E & 1) * 1024;
        if constexpr (E < 2) glds<0>(xb + J * 128, E ? vx1 : vx0, ldw + SL); else glds<0>(wa + (size_t)(J * 64 + (E & 1) * 8) * 2688, vc, ldw + SL); };
    u64 tag; poll_issue(tag, ws + WS_F, 0, 1);
    {
        const int hd = 2 * w + h, sg = (lane >> 2) & 7; const unsigned o = (lane & 3) == 0 ? 0x3F803F80u : 0u;
#pragma unroll
        for (int i = 0; i < 2; ++i) gst16(ws + WS_V + (size_t)(R.rowt * 4 + 2 * i) * 2048, hd * 131072 + sg * 512 + (h ? 0 : 448) + (lane & 3) * 16, (u32x4){o, o, o, o});
    }
    if constexpr (QK) glds<0>(gw, min(lane, 55) * 16, lds0 + 131072 + tb * 1024);
    static_for<4>([&](auto I) { issue(I); });
    __builtin_amdgcn_s_barrier();
    static_for<8>([&](auto I) { issue(std::integral_constant<int, 4 + I>{}); });
    unsigned tch2 = 0;
    const int t = 32 * tb + (lane & 31), gx = (t >> 1) & 7;
    const int blk = (lane >> 4) & 1, q = (lane & 15) >> 2, p = lane & 3;
    unsigned xa[4], wad[2];
#pragma unroll
    for (int s = 0; s < 4; ++s) xa[s] = lds0 + t * 128 + (((2 * s + h) ^ gx) << 4);
#pragma unroll
    for (int a = 0; a < 2; ++a) wad[a] = lds0 + (QK ? 8192 : 0) + (8 * h + q) * 128 + (((4 * a + 2 * blk + (p >> 1)) ^ ((q & 2) << 1)) << 4) + (p & 1) * 8;
    bf16x8 xf[3], wf[3][3];
    f32x16 acc[3];
    auto readf = [&](auto MS_) { constexpr int MS = MS_, B = MS % 3, G = 2 * (MS / 4), O1 = ((G + 1) % 8) * 16384 + (MS & 3) * 2048;
        unsigned a = xa[MS & 3] + (G % 8) * 16384, b0 = wad[0] + O1, b1 = wad[1] + O1; PIN(a); PIN(b0); PIN(b1);
        xf[B] = lds16((lptr)(size_t)a); wf[B][0] = ldstr((lptr)(size_t)b0); wf[B][1] = ldstr((lptr)(size_t)b1);
        if constexpr (QK) wf[B][2] = lds16(lds + 131072 + tb * 1024 + MS * 32 + h * 16); };
    wait_bar_v<pc_wait(0)>();
    PIN(tag); tag = uniform64(tag) + 1;
    readf(std::integral_constant<int, 0>{}); readf(std::integral_constant<int, 1>{});
    static_for<28>([&](auto MS_) { constexpr int MS = MS_, B = MS % 3;
        SBAR();
        if constexpr (MS % 4 == 2 && MS < 24) { static_assert(pc_wait(MS / 4 + 1) >= 0, "phase 1 schedule"); wait_bar_v<pc_wait(MS / 4 + 1)>(); }
        if constexpr (MS == 0) tch2 = warm_code(code, w, lane);
        if constexpr (MS == 23) PIN(tch2);
        if constexpr (MS + 2 < 28) readf(std::integral_constant<int, MS + 2>{});
        SBAR();
        const f32x16 z = {};
        acc[0] = QK ? MFMA(wf[B][0], xf[B], MS == 0 ? z : acc[0]) : MFMA(xf[B], wf[B][0], MS == 0 ? z : acc[0]); SBAR();
        if constexpr (pc_piece_at(MS) >= 0) { issue(std::integral_constant<int, pc_piece_at(MS)>{}); SBAR(); }
        acc[1] = QK ? MFMA(wf[B][1], xf[B], MS == 0 ? z : acc[1]) : MFMA(xf[B], wf[B][1], MS == 0 ? z : acc[1]);
        if constexpr (QK) acc[2] = MFMA(wf[B][2], xf[B], MS == 0 ? z : acc[2]);
    });
    const int tblk = R.rowt * 4 + tb;
    if constexpr (QK) {
#pragma unroll
        for (int g = 0; g < 8; ++g) store_qk(ws, false, qk_off(false, tblk, 48 + g), lane, acc[g >> 2][4 * (g & 3)], acc[g >> 2][4 * (g & 3) + 1], acc[g >> 2][4 * (g & 3) + 2], acc[g >> 2][4 * (g & 3) + 3]);
        const float gv = __builtin_amdgcn_rcpf(1.f + __builtin_amdgcn_exp2f(-1.4426950408889634f * acc[2][0]));
        if (lane < 32) __hip_atomic_store((GAS float*)(ws + WS_G) + tblk * 32 + lane, gv, __ATOMIC_RELAXED, __HIP_MEMORY_SCOPE_AGENT);
    } else {
#pragma unroll
        for (int a = 0; a < 2; ++a) { store_v<0>(ws, tblk, 384 + a * 32 + (lane & 31), h, acc[a]); store_v<1>(ws, tblk, 384 + a * 32 + (lane & 31), h, acc[a]); }
    }
    wait_bar<0>();
    if (threadIdx.x == 0) flag_set(ws, F_QKV + 240 + R.rowt, tag);
    unsigned t0 = 0, t1 = 0;
    {
        const int ln = (R.rowt * 512 + (int)threadIdx.x) * 2; if (ln < 15680) { const char* a = ln < 12544 ? wd + ln * 128 : wo + (ln - 12544) * 128; t0 = touch(a); t1 = touch(a + 128); } }
    u64 fl; const unsigned foff = p2_flag_off(R.head, w, lane);
    poll_issue(fl, ws + WS_F, foff, tag); poll_check<0>(fl, ws + WS_F, foff, tag); PIN(t0); PIN(t1);
    p2_first_tiles(ws, R, w, lane * 16, lds0);
    wait_bar<0>();
    return tag;
}

DI float swap_max(float m) { auto rr = __builtin_amdgcn_permlane32_swap(__float_as_uint(m), __float_as_uint(m), false, false); return __builtin_fmaxf(__uint_as_float(rr[0]), __uint_as_float(rr[1])); }
DI float max16(const f32x16& s) {
    float a = __builtin_fmaxf(__builtin_fmaxf(s[0], s[1]), s[2]), b = __builtin_fmaxf(__builtin_fmaxf(s[3], s[4]), s[5]);
    a = __builtin_fmaxf(__builtin_fmaxf(a, s[6]), s[7]); b = __builtin_fmaxf(__builtin_fmaxf(b, s[8]), s[9]);
    a = __builtin_fmaxf(__builtin_fmaxf(a, s[10]), s[11]); b = __builtin_fmaxf(__builtin_fmaxf(b, s[12]), s[13]);
    a = __builtin_fmaxf(__builtin_fmaxf(a, s[14]), s[15]); return __builtin_fmaxf(a, b);
}
DI void set_ref(bf16x8& q1, float m, int h) {
    const unsigned nb = __float_as_uint(-m), hi = nb & 0xFFFF0000u;
    const float r1 = -m - __uint_as_float(hi); const unsigned mid = __float_as_uint(r1) & 0xFFFF0000u;
    const float r2 = r1 - __uint_as_float(mid); const unsigned lo = __float_as_uint(r2) >> 16;
    u32x4 u = __builtin_bit_cast(u32x4, q1);
    if (h) { u[2] = (hi >> 16) | mid; u[3] = lo; }
    q1 = __builtin_bit_cast(bf16x8, u);
}
DI void exp_pack(const f32x16& s, u32x4& p0, u32x4& p1) {
#pragma unroll
    for (int i = 0; i < 4; ++i) p0[i] = cvtpk(__builtin_amdgcn_exp2f(s[2 * i]), __builtin_amdgcn_exp2f(s[2 * i + 1]));
#pragma unroll
    for (int i = 0; i < 4; ++i) p1[i] = cvtpk(__builtin_amdgcn_exp2f(s[8 + 2 * i]), __builtin_amdgcn_exp2f(s[9 + 2 * i]));
}
#define BC8(x) __builtin_bit_cast(bf16x8, x)

template <bool DOWN, int KH>
DI void phase2(const char* wd, const char* wo, char* ws, char* out, const Role& R, const u64 tag, lptr lds) {
    int tid = threadIdx.x; PIN(tid);
    const int lane = tid & 63, w = __builtin_amdgcn_readfirstlane(tid >> 6);
    const int h = lane >> 5, nt = w & 1, mt = (w >> 1) & 1, head = R.head, rt = R.rt, ct = R.ct;
    const unsigned lds0 = (unsigned)(size_t)lds;
    const int r = lane >> 3, pos = lane & 7;
    const unsigned vlin = lane * 16, vwd = r * 896 + ((pos ^ ((r & 2) << 1)) << 4), v1 = w < 6 ? vlin : vwd, vwd5 = vwd + 64 * 896;
    const char* wsrc = wd + ct * 128;
    const char* wos = wo + ct * 128 + (size_t)w * 8 * 896;
    const u32x4 rws = make_srd(ws), rwd = make_srd(wsrc), r1 = w < 6 ? rws : rwd;
    unsigned skv = (KH ? WS_V : WS_K) + (head * 64 + 32 * mt + nt) * 2048 + 2 * 4096;
    unsigned s0 = WS_H + (2 * rt * 112 + (w / 7) * 112 + (w % 7)) * 1024 - 7168;
    unsigned s1 = w < 6 ? WS_H + (2 * rt * 112 + 112 + w + 1) * 1024 - 7168 : (w - 6) * 8 * 896 - 112 * 896;
    unsigned s2 = (w + 2) * 8 * 896 - 112 * 896;
    const unsigned inc1 = w < 6 ? 7168 : 112 * 896;
    const unsigned kl0 = lds0 + L_KV + w * 2048, dl0 = lds0 + w * 1024;
    unsigned kl = kl0, dl = dl0, uk = 16384, ud = 28672;
    auto advance1 = [&]() { skv += 4096; s0 += 7168; s1 += inc1; s2 += 112 * 896; kl = kl0 + uk; PINS(skv); PINS(s0); PINS(s1); PINS(s2); PINS(kl); };
    auto advance2 = [&]() { dl = dl0 + ud; const bool wrap = uk == 32768; uk = wrap ? 0 : uk + 16384; ud = wrap ? 0 : ud + 28672; PINS(dl); PINS(uk); PINS(ud); };
    auto piece = [&](auto N_, auto KVM_, auto DD_, auto PART_) { constexpr int N = N_, KVM = KVM_, PART = PART_; constexpr bool DD = DD_ && DOWN;
        if constexpr (KVM == 1 && N == 0) { if constexpr (PART == 0) set_m0<0>(kl); else blds<0, true>(rws, vlin, skv); }
        if constexpr (KVM == 1 && N == 1 && PART == 1) blds<1024, true>(rws, vlin, skv);
        if constexpr (PART == 1) {
            if constexpr (DOWN && KVM == 2 && N == 0) { glds<0>(wos, vwd, lds0 + L_WO + w * 1024); const int e = w < 2 ? 8 : 40; glds<0>(wos + (size_t)e * 8 * 896, vwd, lds0 + L_WO + (w + e) * 1024); }
            if constexpr (DOWN && KVM == 2 && N == 1) glds<0>(wos + (size_t)48 * 8 * 896, vwd, lds0 + L_WO + (w + 48) * 1024);
            if constexpr (DOWN && KVM == 3 && N < 2) glds<0>(wos + (size_t)(10 + 8 * N) * 8 * 896, vwd, lds0 + L_WO + (w + 10 + 8 * N) * 1024); }
        if constexpr (DD && N >= 2 && N < 6 - KH && PART == 0) set_m0<(N - 2) * 8192>(dl);
        if constexpr (DD && N == 2 && PART == 1) blds<0, true>(rws, vlin, s0);
        if constexpr (DD && N == 3 && PART == 1) blds<0, true>(r1, v1, s1);
        if constexpr (DD && N == 4 && PART == 1) blds<0, false>(rwd, vwd, s2);
        if constexpr (DD && N == 5 && KH == 0 && PART == 1) blds<0, false>(rwd, vwd5, s2); };
    auto stage_b = [&](int j, unsigned slot) {
        const int v = w - 4;
#pragma unroll
        for (int n = 0; n < 7; ++n) { const int e = v + 4 * n;
            if (n < 3 || (n == 3 && v < 2)) glds_sc1<0>(ws + WS_H + (size_t)((2 * rt + e / 7) * 112 + 7 * j + e % 7) * 1024, vlin, lds0 + slot + e * 1024);
            else glds<0>(wd + ct * 128 + (size_t)(112 * j + (e - 14) * 8) * 896, vwd, lds0 + slot + e * 1024); } };
    constexpr int NV = DOWN ? 6 - KH : 2;
    const int blk = (lane >> 4) & 1, q = (lane & 15) >> 2, p = lane & 3;
    const unsigned kv0 = lds0 + L_KV + KH * 4096 + vlin, hf0 = lds0 + (mt * 7 + KH * 4) * 1024 + vlin;
    const unsigned wd0 = lds0 + 14336 + KH * 8192 + (4 * h + q) * 128 + (((4 * nt + 2 * blk + (p >> 1)) ^ ((q & 2) << 1)) << 4) + (p & 1) * 8;
    unsigned kv = kv0 + 16384, hfp = hf0, wdp = wd0;
    const f32x16 z = {};
    f32x16 o = z, dacc = z, sa, sb; u32x4 pa0, pa1, pb0, pb1; float mref;
    bf16x8 kf[4], vf[4], dw[4], dh[4];
    dw[0] = dw[1] = dh[0] = dh[1] = (bf16x8){0, 0, 0, 0, 0, 0, 0, 0};
    u64 fl = 0;
    const unsigned fa = (lane & 15) < 13 ? (F_HA + (rt >> 1) * 16 + (lane & 15)) * 8 : (F_QKV + 240 + (rt >> 1)) * 8;
    const unsigned fb = (F_HB + (rt >> 1) * 16 + min(lane & 15, 14)) * 8;
#define IC(N) std::integral_constant<int, N>{}
#define LP(a) ((lptr)(size_t)(a))
#define RD_D(F) do { if constexpr (DOWN && DM && (F) < 4 - KH) { dw[F] = ldstr2(LP(wdp) + (F) * 2048); dh[F] = lds16(LP(hfp) + (F) * 1024); } } while (0)
#define MM_D(F) do { SBAR(); if constexpr (DOWN && DM && (F) < 4 - KH) dacc = MFMA(dw[F], dh[F], dacc); SBAR(); } while (0)
#define MM(D, A, B, C) do { SBAR(); D = MFMA(A, B, C); SBAR(); } while (0)
#define EX(S, I) do { float v_ = __builtin_amdgcn_exp2f(S[I]); PIN(v_); S[I] = v_; SBAR(); } while (0)
#define CV(S, I, P) do { unsigned v_ = cvtpk(S[2 * (I)], S[2 * (I) + 1]); PIN(v_); P[(I) & 3] = v_; SBAR(); } while (0)
#define M0S(N) do { piece(IC(N), IC(KVM), IC(DD), IC(0)); SBAR(); } while (0)
#define DMA(N) do { piece(IC(N), IC(KVM), IC(DD), IC(1)); SBAR(); } while (0)
#define XTILE(S, P0, P1, MID) do { EX(S, 0); EX(S, 1); EX(S, 2); EX(S, 3); EX(S, 4); CV(S, 0, P0); EX(S, 5); EX(S, 6); CV(S, 1, P0); EX(S, 7); EX(S, 8); CV(S, 2, P0); EX(S, 9); \
        EX(S, 10); CV(S, 3, P0); EX(S, 11); MID; EX(S, 12); CV(S, 4, P1); EX(S, 13); EX(S, 14); CV(S, 5, P1); EX(S, 15); CV(S, 6, P1); CV(S, 7, P1); } while (0)
#define XPHASE() do { SBAR(); __builtin_amdgcn_s_setprio(0); SBAR(); XTILE(sa, pa0, pa1, do { SBAR(); __builtin_amdgcn_s_setprio(1); SBAR(); } while (0)); XTILE(sb, pb0, pb1, do {} while (0)); SBAR(); } while (0)
#define RD_K() do { kf[0] = lds16(LP(kv)); kf[1] = lds16(LP(kv) + 1024); kf[2] = lds16(LP(kv) + 2048); kf[3] = lds16(LP(kv) + 3072); SBAR(); } while (0)
#define RD_V() do { vf[0] = lds16(LP(kv) + 8192); vf[1] = lds16(LP(kv) + 9216); vf[2] = lds16(LP(kv) + 10240); vf[3] = lds16(LP(kv) + 11264); SBAR(); } while (0)
#define RD_V2(J) do { vf[J] = lds16(LP(kv) + 8192 + (J) * 1024); vf[(J) + 1] = lds16(LP(kv) + 9216 + (J) * 1024); SBAR(); } while (0)
#define NEXT_ADDR() do { kv = kv0 + uk; PIN(kv); if constexpr (DOWN) { hfp = hf0 + ud; wdp = wd0 + ud; PIN(hfp); PIN(wdp); } } while (0)
#define YPHASE(PRE_H, POST_H, LATE) do { SBAR(); __builtin_amdgcn_s_setprio(3); SBAR(); \
        MM(o, vf[0], BC8(pa0), o); RD_K(); \
        MM(o, vf[1], BC8(pa1), o); RD_D(2); SBAR(); M0S(0); \
        MM(o, vf[2], BC8(pb0), o); DMA(0); RD_D(3); \
        MM(o, vf[3], BC8(pb1), o); DMA(1); RD_V2(0); PRE_H; M0S(2); \
        MM_D(0); if constexpr (!(DOWN && DM)) asm volatile("s_nop 0"); DMA(2); POST_H; RD_V2(2); M0S(3); \
        MM_D(1); if constexpr (!(DOWN && DM)) asm volatile("s_nop 0"); DMA(3); RD_D(0); SBAR(); M0S(4); \
        MM(sa, kf[0], q0, z); DMA(4); RD_D(1); SBAR(); M0S(5); \
        MM(sb, kf[2], q0, z); DMA(5); advance1(); \
        MM(sa, kf[1], q1, sa); advance2(); \
        if constexpr (!(DOWN && DM)) { NEXT_ADDR(); LATE; } \
        MM(sb, kf[3], q1, sb); \
        if constexpr (DOWN && DM) { if constexpr (KH == 0) MM_D(2); NEXT_ADDR(); LATE; MM_D(3 - KH); } } while (0)
#define NOP_ do {} while (0)
#define INTERVAL(WAIT, PRE_X, PRE_H, POST_H, LATE) do { if constexpr (KH == 0) { WAIT; PRE_X; XPHASE(); } else { PRE_X; XPHASE(); WAIT; } YPHASE(PRE_H, POST_H, LATE); } while (0)
    if (tid < 4) *(LAS unsigned*)((LAS char*)lds + L_CNT + tid * 4) = 0;
    const bf16x8 q0 = lds16(lds + L_Q + (w & 3) * 2048 + vlin); bf16x8 q1 = lds16(lds + L_Q + (w & 3) * 2048 + 1024 + vlin);
    {
        constexpr int KVM = 1, DD = 0; constexpr bool DM = false;
        M0S(0); RD_K(); RD_V();
        DMA(0); DMA(1); advance1(); advance2(); NEXT_ADDR();
        sa = MFMA(kf[0], q0, z); sa = MFMA(kf[1], q1, sa); sb = MFMA(kf[2], q0, z);
        mref = swap_max(max16(sa)); set_ref(q1, mref, h);
        sb = MFMA(kf[3], q1, sb);
#pragma unroll
        for (int i = 0; i < 16; ++i) sa[i] -= mref;
        SBAR();
    }
    {
        constexpr int KVM = 1, DD = 1; constexpr bool DM = false;
        INTERVAL(wait_bar<((DOWN && KH) ? 3 : 2)>(), if constexpr (DOWN) poll_issue(fl, ws + WS_F, fa, tag), if constexpr (DOWN) poll_check<2>(fl, ws + WS_F, fa, tag),
                 if (KH == 1 && tid == 256 && R.grp < 15) flag_set(ws, F_HB + R.rowt * 16 + R.grp, tag), NOP_);
        INTERVAL(wait_bar<NV>(), NOP_, NOP_, NOP_, NOP_);
    }
    int i = 3;
#pragma unroll 1
    do {
        constexpr int KVM = 1, DD = 1; constexpr bool DM = true;
        INTERVAL(wait_bar<NV>(), NOP_, if (DOWN && __builtin_expect(i == 8, 0)) poll_check<2>(fl, ws + WS_F, fb, tag), NOP_,
                 do { if (DOWN && __builtin_expect(i == 7, 0)) poll_issue(fl, ws + WS_F, fb, tag); ++i; PINS(i); } while (0));
    } while (i < 14);
    { constexpr int KVM = 2, DD = 1; constexpr bool DM = true; INTERVAL(wait_bar<NV>(), NOP_, NOP_, NOP_, NOP_); }
    { constexpr int KVM = 3, DD = 1; constexpr bool DM = true; INTERVAL(wait_bar<(DOWN ? 7 - KH : 0)>(), NOP_, NOP_, NOP_, NOP_); }
    if constexpr (DOWN) {
        __builtin_amdgcn_s_waitcnt(NV | 0x70); asm volatile("" ::: "memory"); if (lane == 0) (void)lds_count((LAS char*)lds + L_CNT + 4); }
    XPHASE();
    LAS unsigned* cnt = (LAS unsigned*)((LAS char*)lds + L_CNT);
    __builtin_amdgcn_s_setprio(3);
    LAS char* sco = (LAS char*)lds + ((w & 2) ? L_Q : L_SC) + (w & 1) * 4096; LAS char* scm = (LAS char*)lds + L_SC + 8192 + (w & 3) * 256;
    o = MFMA(vf[0], BC8(pa0), o); o = MFMA(vf[1], BC8(pa1), o); o = MFMA(vf[2], BC8(pb0), o); o = MFMA(vf[3], BC8(pb1), o);
    f32x4 pv[4]; float m2 = 0.f;
    if constexpr (KH == 0) {
#pragma unroll 1
        for (unsigned spins = 0; __hip_atomic_load(cnt + 2, __ATOMIC_RELAXED, __HIP_MEMORY_SCOPE_WORKGROUP) < 4u && spins < (1u << 22); ++spins) __builtin_amdgcn_s_sleep(1);
        asm volatile("" ::: "memory");
        m2 = *(LAS float*)(scm + lane * 4);
#pragma unroll
        for (int g = 0; g < 4; ++g) pv[g] = *(LAS f32x4*)(sco + g * 1024 + vlin);
        if constexpr (DOWN) { asm volatile("s_waitcnt vmcnt(0)" ::: "memory"); if (lane == 0) (void)lds_count((LAS char*)(cnt + 3)); }
    }
    {
        float c0 = ((head & 1) ? o[0] : o[12]) * 0x1p32f, c1 = 0.f, c2 = 0.f, c3 = 0.f; if (h == (head & 1)) c0 = 0.f;
#pragma unroll
        for (int i = 0; i < 4; ++i) { c0 = __builtin_fmaf(o[i], 0.f, c0); c1 = __builtin_fmaf(o[4 + i], 0.f, c1); c2 = __builtin_fmaf(o[8 + i], 0.f, c2); c3 = __builtin_fmaf(o[12 + i], 0.f, c3); }
        if (__builtin_expect(__any(!(__builtin_fabsf((c0 + c1) + (c2 + c3)) <= 3.0e38f)), 0)) {
            const char* kg = ws + WS_K + (size_t)(head * 64 + 32 * KH) * 2048; const char* vg = ws + WS_V + (size_t)(head * 64 + 32 * KH) * 2048;
            u32x4 ld[4], p0, p1;
            set_ref(q1, 0.f, h);
            float m = -3.0e38f;
#pragma unroll 1
            for (int n = 0; n < 16; ++n) { gld4_sc1(ld, kg + n * 4096, vlin); wait4<0>(ld);
                sa = MFMA(BC8(ld[0]), q0, z); sa = MFMA(BC8(ld[1]), q1, sa); sb = MFMA(BC8(ld[2]), q0, z); sb = MFMA(BC8(ld[3]), q1, sb);
                m = __builtin_fmaxf(m, __builtin_fmaxf(max16(sa), max16(sb))); }
            mref = swap_max(m); set_ref(q1, mref, h); o = z;
#pragma unroll 1
            for (int n = 0; n < 16; ++n) { gld4_sc1(ld, kg + n * 4096, vlin); wait4<0>(ld);
                sa = MFMA(BC8(ld[0]), q0, z); sa = MFMA(BC8(ld[1]), q1, sa); sb = MFMA(BC8(ld[2]), q0, z); sb = MFMA(BC8(ld[3]), q1, sb);
                gld4_sc1(ld, vg + n * 4096, vlin); wait4<0>(ld);
                exp_pack(sa, p0, p1); o = MFMA(BC8(ld[0]), BC8(p0), o); o = MFMA(BC8(ld[1]), BC8(p1), o);
                exp_pack(sb, p0, p1); o = MFMA(BC8(ld[2]), BC8(p0), o); o = MFMA(BC8(ld[3]), BC8(p1), o); }
        }
    }
    if constexpr (KH == 1) {
#pragma unroll
        for (int g = 0; g < 4; ++g) *(LAS f32x4*)(sco + g * 1024 + vlin) = (f32x4){o[4 * g], o[4 * g + 1], o[4 * g + 2], o[4 * g + 3]};
        *(LAS float*)(scm + lane * 4) = mref;
        __builtin_amdgcn_s_waitcnt(0xC07F);
        if (lane == 0) (void)lds_count((LAS char*)(cnt + 2));
        if constexpr (DOWN) {
            __builtin_amdgcn_s_setprio(0);
#pragma unroll 1
            for (unsigned spins = 0; __hip_atomic_load(cnt + 1, __ATOMIC_RELAXED, __HIP_MEMORY_SCOPE_WORKGROUP) < 8u && spins < (1u << 22); ++spins) __builtin_amdgcn_s_sleep(1);
            asm volatile("" ::: "memory");
            stage_b(15, 28672);
#pragma unroll
            for (int n = 0; n < 4; ++n) glds<0>(wo + ct * 128 + (size_t)(22 + w + 4 * n) * 8 * 896, vwd, lds0 + L_WO + (22 + w + 4 * n) * 1024);
            { constexpr bool DM = true; MM_D(0); MM_D(1); }
            static_for<3>([&](auto J) { constexpr unsigned uu = (J + 2) % 3;
                if constexpr (J > 0) {
                    asm volatile("s_waitcnt vmcnt(%0)" ::"i"(J == 1 ? 11 : 0) : "memory"); if (lane == 0) (void)lds_count((LAS char*)(cnt + 3));
#pragma unroll 1
                    for (unsigned spins = 0; __hip_atomic_load(cnt + 3, __ATOMIC_RELAXED, __HIP_MEMORY_SCOPE_WORKGROUP) < 4u + 4u * J && spins < (1u << 22); ++spins) __builtin_amdgcn_s_sleep(1);
                    asm volatile("" ::: "memory"); }
                bf16x8 ew[7], eh[7];
#pragma unroll
                for (int f = 0; f < 7; ++f) { ew[f] = ldstr2(LP(wd0 - 8192 + uu * 28672) + f * 2048); eh[f] = lds16(LP(hf0 - 4096 + uu * 28672) + f * 1024); }
                SBAR();
#pragma unroll
                for (int f = 0; f < 7; ++f) dacc = MFMA(ew[f], eh[f], dacc);
                SBAR(); });
        }
    } else {
        const float ms = __builtin_fmaxf(mref, m2);
        const float f1 = __builtin_amdgcn_exp2f(mref - ms), f2 = __builtin_amdgcn_exp2f(m2 - ms);
#pragma unroll
        for (int i = 0; i < 16; ++i) o[i] = o[i] * f1 + pv[i >> 2][i & 3] * f2;
        const unsigned lv = __float_as_uint((head & 1) ? o[0] : o[12]);
        auto rr = __builtin_amdgcn_permlane32_swap(lv, lv, false, false);
        const float rl = __builtin_amdgcn_rcpf(__uint_as_float((head & 1) ? rr[0] : rr[1]));
        u32x4 a, b;
#pragma unroll
        for (int i = 0; i < 4; ++i) { a[i] = cvtpk(o[2 * i] * rl, o[2 * i + 1] * rl); b[i] = cvtpk(o[8 + 2 * i] * rl, o[9 + 2 * i] * rl); }
        if (head & 1) { if (!h) { a[0] = 0; a[1] = 0; } } else if (h) { b[2] = 0; b[3] = 0; }
        char* ob = ws + WS_O + (size_t)((R.qb * 4 + w) * 16 + head) * 2048;
        gst16(ob, vlin, a); gst16(ob + 1024, vlin, b);
        asm volatile("s_waitcnt vmcnt(0)" ::: "memory");
        if (lane == 0) flag_set(ws, F_O + (R.qb * 4 + w) * 16 + head, tag);
    }
    if constexpr (DOWN) {
        if constexpr (KH == 0) { constexpr bool DM = true; MM_D(0); MM_D(1); }
        wait_bar<0>();
        const unsigned fo = (F_O + (2 * rt + mt) * 16 + 8 * KH + (lane & 7)) * 8;
        poll_issue(fl, ws + WS_F, fo, tag);
        float g; asm volatile("global_load_dword %0, %1, %2 sc1" : "=&v"(g) : "v"((lane & 31) * 4), "s"(ws + WS_G + (rt * 64 + mt * 32) * 4) : "memory");
        bf16x8 wf[16];
        const lptr wo0 = lds + L_WO + (KH * 224 + 4 * h + q) * 128 + (((4 * nt + 2 * blk + (p >> 1)) ^ ((q & 2) << 1)) << 4) + (p & 1) * 8;
#pragma unroll
        for (int s = 0; s < 16; ++s) wf[s] = ldstr2(wo0 + ((s >> 1) * 28 - ((s >> 1) & 1) * 4 + (s & 1) * 16) * 128);
        poll_check<0, 0>(fl, ws + WS_F, fo, tag);
        PIN(g);
        u32x4 of[16];
        const char* os = ws + WS_O + (size_t)(2 * rt + mt) * 32768 + KH * 16384;
#pragma unroll
        for (int i = 0; i < 4; ++i) gld4_sc1(of + 4 * i, os + i * 4096, vlin);
#pragma unroll
        for (int i = 0; i < 16; ++i) dacc[i] *= 1.f - g;
        f32x16 acc = z;
        static_for<4>([&](auto I) {
            wait4<12 - 4 * I>(of + 4 * I);
#pragma unroll
            for (int j = 0; j < 4; ++j) acc = MFMA(wf[4 * I + j], BC8(of[4 * I + j]), acc);
            SBAR(); });
        LAS char* ot = (LAS char*)lds + KH * 16384;
        const int tok = mt * 32 + (lane & 31);
#pragma unroll
        for (int i = 0; i < 4; ++i)
            *(LAS f32x4*)(ot + tok * 256 + (((nt * 8 + 2 * i + h) ^ (tok & 15)) << 4)) = (f32x4){g * acc[4 * i] + dacc[4 * i], g * acc[4 * i + 1] + dacc[4 * i + 1], g * acc[4 * i + 2] + dacc[4 * i + 2], g * acc[4 * i + 3] + dacc[4 * i + 3]};
        wait_bar<0>();
        const int row = tid >> 3;
        const lptr rp = lds + row * 256; const int c0 = ((2 * pos) ^ (row & 15)) << 4, c1 = ((2 * pos + 1) ^ (row & 15)) << 4;
        const f32x4 s0 = *(const LAS f32x4*)(rp + c0) + *(const LAS f32x4*)(rp + 16384 + c0), s1 = *(const LAS f32x4*)(rp + c1) + *(const LAS f32x4*)(rp + 16384 + c1);
        const u32x4 ov = {cvtpk(s0[0], s0[1]), cvtpk(s0[2], s0[3]), cvtpk(s1[0], s1[1]), cvtpk(s1[2], s1[3])};
        asm volatile("global_store_dwordx4 %0, %1, %2" ::"v"(row * 896 + pos * 16), "v"(ov), "s"(out + (size_t)rt * 64 * 896 + ct * 128) : "memory");
    } else if (blockIdx.x == 255 && w == 7) {
#pragma unroll
        for (int j = 0; j < 4; ++j) { poll_issue(fl, ws + WS_F, (F_QKV + j * 64 + lane) * 8, tag); poll_check<0>(fl, ws + WS_F, (F_QKV + j * 64 + lane) * 8, tag); }
        if (lane == 0) flag_set(ws, 0, tag);
    }
}

__global__ __launch_bounds__(512, 2) void fused(const char* x, const char* wqkv, const char* wo, const char* wg, const char* wu, const char* wd, const char* gw, char* ws, char* out) {
    extern __shared__ __attribute__((aligned(16))) char smem[];
    const int b = blockIdx.x, xcd = b & 7, idx = b >> 3;
    Role R; R.rowt = (xcd >> 1) * 4 + (idx & 3); R.grp = 8 * (xcd & 1) + (idx >> 2); R.head = 2 * xcd + (idx >> 4); R.qb = idx & 15; R.rt = 4 * xcd + idx / 7; R.ct = idx % 7;
    const char* code; asm volatile("s_getpc_b64 %0" : "=s"(code));
    const bool hi = __builtin_amdgcn_readfirstlane(threadIdx.x) >= 256;
    u64 tag;
    if (__builtin_expect(R.grp < 13, 1)) tag = phase1_main<false>(x, wqkv, wg, wu, code, ws, R, (lptr)smem);
    else if (__builtin_expect(R.grp < 15, 1)) tag = phase1_main<true>(x, wqkv, wg, wu, code, ws, R, (lptr)smem);
    else if (hi) tag = phase1_last<true>(x, wqkv, gw, wd, wo, code, ws, R, (lptr)smem);
    else tag = phase1_last<false>(x, wqkv, gw, wd, wo, code, ws, R, (lptr)smem);
    if (__builtin_expect(idx < 28, 1)) { if (hi) phase2<true, 1>(wd, wo, ws, out, R, tag, (lptr)smem); else phase2<true, 0>(wd, wo, ws, out, R, tag, (lptr)smem); }
    else { if (hi) phase2<false, 1>(wd, wo, ws, out, R, tag, (lptr)smem); else phase2<false, 0>(wd, wo, ws, out, R, tag, (lptr)smem); }
}

extern "C" void kernel_launch(void* const* d_in, const int* in_sizes, int n_in, void* d_out, int out_size, void* d_ws, size_t ws_size, hipStream_t stream) {
    (void)in_sizes; (void)n_in; (void)out_size; (void)ws_size;
    (void)hipFuncSetAttribute((const void*)fused, hipFuncAttributeMaxDynamicSharedMemorySize, L_END);
    fused<<<256, 512, L_END, stream>>>((const char*)d_in[0], (const char*)d_in[1], (const char*)d_in[2], (const char*)d_in[4], (const char*)d_in[5], (const char*)d_in[6], (const char*)d_in[7], (char*)d_ws, (char*)d_out);
}
```
